```python
import torch

def _softmax(x, axis=-1):
    return torch.softmax(x, dim=axis)
def _gelu(x):
    return torch.nn.functional.gelu(x, approximate="tanh")
def _silu(x):
    return torch.nn.functional.silu(x)
def _sigmoid(x):
    return torch.sigmoid(x)
def _layernorm(x, axis=-1, eps=1e-5):
    m = x.mean(dim=axis, keepdim=True)
    v = x.var(dim=axis, keepdim=True, unbiased=False)
    return (x - m) / torch.sqrt(v + eps)
def _scatter_add(tgt, idx, src, axis=0):
    return tgt.clone().index_add_(axis, idx, src)
def _segment_sum(src, seg, n):
    out = torch.zeros((n,) + src.shape[1:], dtype=src.dtype)
    return out.index_add_(0, seg, src)
def _pad(x, pw):
    flat = tuple(v for pair in reversed(pw) for v in pair)
    return torch.nn.functional.pad(x, flat)

kernel_name = 'standalone__mha_block_cached__near_ridge'

def setup_inputs():

    _w = (lambda a: (lambda t: t.to(torch.bfloat16) if t.is_floating_point() else t)(torch.as_tensor(a)))
    B, Sq, D = 8, 128, 512
    H, Sk, Dh = 32, 128, 128
    return {
        "x": _w((lambda *s: torch.randn(*s, dtype=torch.bfloat16) * 0.8)(B, Sq, D)),
        "k_cache": _w((lambda *s: torch.randn(*s, dtype=torch.bfloat16) * 0.8)(B, H, Sk, Dh)),
        "v_cache": _w((lambda *s: torch.randn(*s, dtype=torch.bfloat16) * 0.05)(B, H, Sk, Dh)),
        "wq": _w((lambda *s: torch.randn(*s, dtype=torch.bfloat16) * 0.8)(D, H * Dh) * (D ** -0.5)),
        "wo": _w((lambda *s: torch.randn(*s, dtype=torch.bfloat16) * 0.05)(H * Dh, D) * ((H * Dh) ** -0.5)),
    }

def reference(x, k_cache, v_cache, wq, wo):
    B, Sq, D = x.shape
    H, Dh = 32, 128
    q = (lambda x, *d: x.permute(*d))((x @ wq).reshape(B, Sq, H, Dh), 0, 2, 1, 3)
    scores = torch.einsum("bhqd,bhkd->bhqk", q, k_cache) * (Dh ** -0.5)
    p = _softmax(scores, axis=-1)
    o = torch.einsum("bhqk,bhkd->bhqd", p, v_cache)
    return (lambda x, *d: x.permute(*d))(o, 0, 2, 1, 3).reshape(B, Sq, H * Dh) @ wo
```

```cpp
#include <hip/hip_runtime.h>
#include <hip/hip_bf16.h>
typedef unsigned short u16;
__device__ inline float bf2f(u16 v){ return __uint_as_float(((unsigned)v)<<16); }
__device__ inline u16 f2bf(float f){ unsigned u=__float_as_uint(f); u += 0x7fffu + ((u>>16)&1u); return (u16)(u>>16); }

__global__ void k_attn(const u16* x, const u16* k, const u16* v, const u16* wq, u16* o){
  int bh = blockIdx.x; int b = bh>>5, h = bh&31;
  __shared__ float q[128][129];
  __shared__ float s[128][129];
  int t = threadIdx.x;
  for(int i=t;i<128*128;i+=256){ int r=i>>7,c=i&127; float a=0; for(int kk=0;kk<512;kk++) a+=bf2f(x[(b*128+r)*512+kk])*bf2f(wq[kk*4096+h*128+c]); q[r][c]=bf2f(f2bf(a)); }
  __syncthreads();
  for(int i=t;i<128*128;i+=256){ int r=i>>7,c=i&127; float a=0; for(int d=0;d<128;d++) a+=q[r][d]*bf2f(k[((size_t)bh*128+c)*128+d]); s[r][c]=a*0.08838834764f; }
  __syncthreads();
  if(t<128){ float m=-1e30f; for(int c=0;c<128;c++) m=fmaxf(m,s[t][c]); float l=0; for(int c=0;c<128;c++){ float e=__expf(s[t][c]-m); s[t][c]=e; l+=e;} for(int c=0;c<128;c++) s[t][c]=bf2f(f2bf(s[t][c]/l)); }
  __syncthreads();
  for(int i=t;i<128*128;i+=256){ int r=i>>7,c=i&127; float a=0; for(int kk=0;kk<128;kk++) a+=s[r][kk]*bf2f(v[((size_t)bh*128+kk)*128+c]); o[(size_t)(b*128+r)*4096+h*128+c]=f2bf(a); }
}
__global__ void k_out(const u16* o, const u16* wo, u16* out){
  int i = blockIdx.x*256+threadIdx.x; int r=i>>9,c=i&511; float a=0; for(int kk=0;kk<4096;kk++) a+=bf2f(o[(size_t)r*4096+kk])*bf2f(wo[kk*512+c]); out[i]=f2bf(a);
}
extern "C" void kernel_launch(void* const* d_in, const int* in_sizes, int n_in, void* d_out, int out_size, void* d_ws, size_t ws_size, hipStream_t stream) {
  const u16* x=(const u16*)d_in[0]; const u16* k=(const u16*)d_in[1]; const u16* v=(const u16*)d_in[2]; const u16* wq=(const u16*)d_in[3]; const u16* wo=(const u16*)d_in[4];
  u16* o=(u16*)d_ws;
  k_attn<<<256,256,0,stream>>>(x,k,v,wq,o);
  k_out<<<1024*512/256,256,0,stream>>>(o,wo,(u16*)d_out);
}
```

```cpp
#include <hip/hip_runtime.h>
extern "C" __global__ __launch_bounds__(512, 2) void mha_block_kernel(const void* x, const void* kc, const void* vc, const void* wq, const void* wo, void* out, void* ws) {
  __shared__ __attribute__((aligned(16))) char lds[163840];
  unsigned wid = __builtin_amdgcn_readfirstlane(threadIdx.x >> 6);
  unsigned ldsb = (unsigned)(size_t)lds;
  auto ka = __builtin_amdgcn_kernarg_segment_ptr();
  asm volatile(
    "s_load_dwordx8 s[48:55], %2, 0x0\n"
    "s_load_dwordx4 s[56:59], %2, 0x20\n"
    "s_load_dwordx2 s[60:61], %2, 0x30\n"
    "s_mov_b32 s8, %0\n"
    "s_mov_b32 s9, %1\n"
    "s_and_b32 s11, s9, 7\n"
    "s_lshr_b32 s12, s9, 3\n"
    "s_and_b32 s12, s12, 3\n"
    "s_lshr_b32 s10, s9, 5\n"
    "s_lshl_b32 s13, s11, 2\n"
    "s_add_u32 s13, s13, s12\n"
    "s_and_b32 s14, s8, 3\n"
    "s_mov_b32 s64, 0x3e0293ee\n"
    "v_mbcnt_lo_u32_b32 v0, -1, 0\n"
    "v_mbcnt_hi_u32_b32 v0, -1, v0\n"
    "s_mov_b32 s23, 0x20000\n"
    "s_mov_b32 s27, 0x20000\n"
    "s_mov_b32 s31, 0x20000\n"
    "s_mov_b32 s35, 0x20000\n"
    "s_mov_b32 s39, 0x20000\n"
    "s_mov_b32 s43, 0x20000\n"
    "s_mov_b32 s47, 0x20000\n"
    "s_cmp_lt_u32 s8, 4\n"
    "s_cbranch_scc1 L_pathA_%=\n"
    "v_lshrrev_b32 v4, 3, v0\n"
    "v_and_b32 v5, 7, v0\n"
    "v_lshrrev_b32 v6, 4, v0\n"
    "v_xor_b32 v7, v5, v6\n"
    "v_lshlrev_b32 v7, 4, v7\n"
    "v_lshl_add_u32 v16, v4, 10, v7\n"
    "v_xor_b32 v17, 64, v16\n"
    "v_and_b32 v5, 15, v0\n"
    "v_lshlrev_b32 v7, 2, v6\n"
    "v_xor_b32 v7, v7, v5\n"
    "v_lshlrev_b32 v7, 4, v7\n"
    "v_lshl_add_u32 v18, v6, 13, v7\n"
    "s_lshl_b32 s74, s14, 12\n"
    "s_lshl_b32 s75, s14, 17\n"
    "s_lshl_b32 s70, s14, 15\n"
    "s_add_u32 s70, s70, 0\n"
    "s_add_u32 s96, s75, 0\n"
    "s_lshl_b32 s71, s14, 15\n"
    "s_add_u32 s71, s71, 8192\n"
    "s_add_u32 s97, s75, 32768\n"
    "s_lshl_b32 s72, s14, 15\n"
    "s_add_u32 s72, s72, 16384\n"
    "s_add_u32 s98, s75, 65536\n"
    "s_lshl_b32 s73, s14, 15\n"
    "s_add_u32 s73, s73, 24576\n"
    "s_add_u32 s99, s75, 98304\n"
    "s_mul_i32 s88, s12, 3\n"
    "s_lshr_b32 s15, s10, 0\n"
    "s_mul_i32 s15, s15, 1\n"
    "s_add_u32 s88, s88, s15\n"
    "s_cmp_ge_u32 s88, 7\n"
    "s_cselect_b32 s15, 7, 0\n"
    "s_sub_u32 s88, s88, s15\n"
    "s_cmp_ge_u32 s88, 7\n"
    "s_cselect_b32 s15, 7, 0\n"
    "s_sub_u32 s88, s88, s15\n"
    "s_cmp_ge_u32 s88, 7\n"
    "s_cselect_b32 s15, 7, 0\n"
    "s_sub_u32 s88, s88, s15\n"
    "s_waitcnt lgkmcnt(0)\n"
    "; @reset\n"
    "s_lshl_b32 s15, s10, 17\n"
    "s_add_u32 s20, s48, s15\n"
    "s_addc_u32 s21, s49, 0\n"
    "s_and_b32 s21, s21, 0xffff\n"
    "s_sub_u32 s22, 1048576, s15\n"
    "s_lshl_b32 s15, s13, 8\n"
    "s_add_u32 s32, s54, s15\n"
    "s_addc_u32 s33, s55, 0\n"
    "s_and_b32 s33, s33, 0xffff\n"
    "s_sub_u32 s34, 4194304, s15\n"
    "s_mov_b32 s78, 0\n"
    "s_lshl_b32 s79, s78, 19\n"
    "s_lshl_b32 s78, s78, 7\n"
    "s_add_u32 m0, s74, 0\n"
    "s_add_u32 s66, s70, s78\n"
    "buffer_load_dwordx4 v16, s[20:23], s66 offen lds\n"
    "s_add_u32 m0, s74, 1024\n"
    "s_add_u32 s66, s71, s78\n"
    "buffer_load_dwordx4 v17, s[20:23], s66 offen lds\n"
    "s_add_u32 m0, s74, 2048\n"
    "s_add_u32 s66, s72, s78\n"
    "buffer_load_dwordx4 v16, s[20:23], s66 offen lds\n"
    "s_add_u32 m0, s74, 3072\n"
    "s_add_u32 s66, s73, s78\n"
    "buffer_load_dwordx4 v17, s[20:23], s66 offen lds\n"
    "s_add_u32 m0, s74, 16384\n"
    "s_add_u32 s65, s96, s79\n"
    "buffer_load_dwordx4 v18, s[32:35], s65 offen nt lds\n"
    "s_add_u32 m0, s74, 17408\n"
    "s_add_u32 s65, s97, s79\n"
    "buffer_load_dwordx4 v18, s[32:35], s65 offen nt lds\n"
    "s_add_u32 m0, s74, 18432\n"
    "s_add_u32 s65, s98, s79\n"
    "buffer_load_dwordx4 v18, s[32:35], s65 offen nt lds\n"
    "s_add_u32 m0, s74, 19456\n"
    "s_add_u32 s65, s99, s79\n"
    "buffer_load_dwordx4 v18, s[32:35], s65 offen nt lds\n"
    "s_add_u32 s78, s88, 0\n"
    "s_cmp_ge_u32 s78, 7\n"
    "s_cselect_b32 s79, 7, 0\n"
    "s_sub_u32 s78, s78, s79\n"
    "s_add_u32 s78, s78, 1\n"
    "s_lshl_b32 s79, s78, 19\n"
    "s_lshl_b32 s78, s78, 7\n"
    "s_add_u32 m0, s74, 49152\n"
    "s_add_u32 s65, s96, s79\n"
    "buffer_load_dwordx4 v18, s[32:35], s65 offen nt lds\n"
    "s_add_u32 m0, s74, 50176\n"
    "s_add_u32 s65, s97, s79\n"
    "buffer_load_dwordx4 v18, s[32:35], s65 offen nt lds\n"
    "s_add_u32 m0, s74, 51200\n"
    "s_add_u32 s65, s98, s79\n"
    "buffer_load_dwordx4 v18, s[32:35], s65 offen nt lds\n"
    "s_add_u32 m0, s74, 52224\n"
    "s_add_u32 s65, s99, s79\n"
    "buffer_load_dwordx4 v18, s[32:35], s65 offen nt lds\n"
    "s_add_u32 m0, s74, 32768\n"
    "s_add_u32 s66, s70, s78\n"
    "buffer_load_dwordx4 v16, s[20:23], s66 offen lds\n"
    "s_add_u32 m0, s74, 33792\n"
    "s_add_u32 s66, s71, s78\n"
    "buffer_load_dwordx4 v17, s[20:23], s66 offen lds\n"
    "s_add_u32 m0, s74, 34816\n"
    "s_add_u32 s66, s72, s78\n"
    "buffer_load_dwordx4 v16, s[20:23], s66 offen lds\n"
    "s_add_u32 m0, s74, 35840\n"
    "s_add_u32 s66, s73, s78\n"
    "buffer_load_dwordx4 v17, s[20:23], s66 offen lds\n"
    "s_lshl_b32 s15, s10, 7\n"
    "s_add_u32 s16, s60, s15\n"
    "s_addc_u32 s17, s61, 0\n"
    "s_load_dwordx2 s[62:63], s[16:17], 0x0\n"
    "s_lshl_b32 s15, s10, 17\n"
    "s_add_u32 s40, s58, s15\n"
    "s_addc_u32 s41, s59, 0\n"
    "s_and_b32 s41, s41, 0xffff\n"
    "s_sub_u32 s42, 1048576, s15\n"
    "s_lshl_b32 s15, s10, 5\n"
    "s_add_u32 s15, s15, s13\n"
    "s_lshl_b32 s15, s15, 15\n"
    "s_add_u32 s24, s50, s15\n"
    "s_addc_u32 s25, s51, 0\n"
    "s_and_b32 s25, s25, 0xffff\n"
    "s_sub_u32 s26, 8388608, s15\n"
    "s_add_u32 s28, s52, s15\n"
    "s_addc_u32 s29, s53, 0\n"
    "s_and_b32 s29, s29, 0xffff\n"
    "s_sub_u32 s30, 8388608, s15\n"
    "s_lshl_b32 s15, s11, 19\n"
    "s_xor_b32 s65, s12, 0\n"
    "s_lshl_b32 s65, s65, 8\n"
    "s_add_u32 s15, s15, s65\n"
    "s_add_u32 s36, s56, s15\n"
    "s_addc_u32 s37, s57, 0\n"
    "s_and_b32 s37, s37, 0xffff\n"
    "s_sub_u32 s38, 4194304, s15\n"
    "s_mov_b32 s44, s60\n"
    "s_and_b32 s45, s61, 0xffff\n"
    "s_mov_b32 s46, 18874368\n"
    "v_lshrrev_b32 v6, 4, v0\n"
    "v_and_b32 v5, 15, v0\n"
    "v_lshlrev_b32 v7, 2, v6\n"
    "v_xor_b32 v7, v7, v5\n"
    "v_lshlrev_b32 v7, 4, v7\n"
    "v_lshl_add_u32 v19, v6, 10, v7\n"
    "v_lshl_add_u32 v13, v6, 8, v7\n"
    "s_lshl_b32 s15, s14, 2\n"
    "v_add_u32 v7, s15, v6\n"
    "v_and_b32 v7, 15, v7\n"
    "v_xor_b32 v7, v7, v5\n"
    "v_lshlrev_b32 v7, 4, v7\n"
    "v_lshl_add_u32 v12, v6, 8, v7\n"
    "s_lshr_b32 s65, s9, 3\n"
    "s_lshl_b32 s65, s65, 2\n"
    "s_add_u32 s65, s65, s14\n"
    "s_lshl_b32 s65, s65, 12\n"
    "s_xor_b32 s66, s12, 0\n"
    "s_lshl_b32 s66, s66, 8\n"
    "s_sub_u32 s65, s65, s66\n"
    "v_lshlrev_b32 v4, 7, v0\n"
    "v_add_u32 v4, s65, v4\n"
    "v_mov_b32 v8, 0x7ffff000\n"
    "v_cmp_gt_u32 vcc, 32, v0\n"
    "s_nop 1\n"
    "v_cndmask_b32 v3, v8, v4, vcc\n"
    "s_lshl_b32 s65, s10, 5\n"
    "s_lshl_b32 s66, s12, 3\n"
    "s_add_u32 s65, s65, s66\n"
    "v_add_u32 v4, s65, v0\n"
    "v_lshlrev_b32 v4, 11, v4\n"
    "s_lshl_b32 s66, s14, 9\n"
    "s_lshr_b32 s67, s11, 1\n"
    "s_lshl_b32 s68, s67, 7\n"
    "s_add_u32 s66, s66, s68\n"
    "s_add_u32 s66, s66, 528384\n"
    "v_add_u32 v4, s66, v4\n"
    "v_mov_b32 v5, 0x7ffff000\n"
    "v_cmp_gt_u32 vcc, 8, v0\n"
    "s_nop 1\n"
    "v_cndmask_b32 v20, v5, v4, vcc\n"
    "s_lshl_b32 s80, s65, 15\n"
    "s_lshl_b32 s68, s67, 13\n"
    "s_add_u32 s80, s80, s68\n"
    "s_lshl_b32 s68, s14, 11\n"
    "s_add_u32 s80, s80, s68\n"
    "s_and_b32 s68, s11, 1\n"
    "s_lshl_b32 s69, s68, 10\n"
    "s_add_u32 s80, s80, s69\n"
    "s_add_u32 s80, s80, 10485760\n"
    "v_and_b32 v21, 31, v0\n"
    "v_lshlrev_b32 v21, 10, v21\n"
    "v_lshrrev_b32 v4, 5, v0\n"
    "v_lshl_add_u32 v21, v4, 4, v21\n"
    "s_lshl_b32 s81, s67, 15\n"
    "s_xor_b32 s69, s12, 0\n"
    "s_lshl_b32 s69, s69, 8\n"
    "s_add_u32 s81, s81, s69\n"
    "s_lshl_b32 s69, s14, 6\n"
    "s_add_u32 s81, s81, s69\n"
    "s_lshl_b32 s69, s68, 5\n"
    "s_add_u32 s81, s81, s69\n"
    "v_lshlrev_b32 v22, 4, v0\n"
    "s_waitcnt lgkmcnt(0)\n"
    "s_add_u32 s62, s62, 1\n"
    "s_addc_u32 s63, s63, 0\n"
    "s_waitcnt vmcnt(8)\n"
    "s_barrier\n"
    "s_add_u32 s78, s88, 1\n"
    "s_cmp_ge_u32 s78, 7\n"
    "s_cselect_b32 s79, 7, 0\n"
    "s_sub_u32 s78, s78, s79\n"
    "s_add_u32 s78, s78, 1\n"
    "s_lshl_b32 s79, s78, 19\n"
    "s_lshl_b32 s78, s78, 7\n"
    "s_add_u32 m0, s74, 65536\n"
    "s_add_u32 s66, s70, s78\n"
    "buffer_load_dwordx4 v16, s[20:23], s66 offen lds\n"
    "s_add_u32 m0, s74, 81920\n"
    "s_add_u32 s65, s96, s79\n"
    "buffer_load_dwordx4 v18, s[32:35], s65 offen lds\n"
    "s_add_u32 m0, s74, 66560\n"
    "s_add_u32 s66, s71, s78\n"
    "buffer_load_dwordx4 v17, s[20:23], s66 offen lds\n"
    "s_add_u32 m0, s74, 82944\n"
    "s_add_u32 s65, s97, s79\n"
    "buffer_load_dwordx4 v18, s[32:35], s65 offen lds\n"
    "s_add_u32 m0, s74, 67584\n"
    "s_add_u32 s66, s72, s78\n"
    "buffer_load_dwordx4 v16, s[20:23], s66 offen lds\n"
    "s_add_u32 m0, s74, 83968\n"
    "s_add_u32 s65, s98, s79\n"
    "buffer_load_dwordx4 v18, s[32:35], s65 offen lds\n"
    "s_add_u32 m0, s74, 68608\n"
    "s_add_u32 s66, s73, s78\n"
    "buffer_load_dwordx4 v17, s[20:23], s66 offen lds\n"
    "s_add_u32 m0, s74, 84992\n"
    "s_add_u32 s65, s99, s79\n"
    "buffer_load_dwordx4 v18, s[32:35], s65 offen lds\n"
    "s_add_u32 s78, s88, 2\n"
    "s_cmp_ge_u32 s78, 7\n"
    "s_cselect_b32 s79, 7, 0\n"
    "s_sub_u32 s78, s78, s79\n"
    "s_add_u32 s78, s78, 1\n"
    "s_lshl_b32 s79, s78, 19\n"
    "s_lshl_b32 s78, s78, 7\n"
    "s_add_u32 m0, s74, 98304\n"
    "s_add_u32 s66, s70, s78\n"
    "buffer_load_dwordx4 v16, s[20:23], s66 offen lds\n"
    "s_add_u32 m0, s74, 99328\n"
    "s_add_u32 s66, s71, s78\n"
    "buffer_load_dwordx4 v17, s[20:23], s66 offen lds\n"
    "s_add_u32 m0, s74, 100352\n"
    "s_add_u32 s66, s72, s78\n"
    "buffer_load_dwordx4 v16, s[20:23], s66 offen lds\n"
    "s_add_u32 m0, s74, 101376\n"
    "s_add_u32 s66, s73, s78\n"
    "buffer_load_dwordx4 v17, s[20:23], s66 offen lds\n"
    "s_waitcnt vmcnt(12)\n"
    "s_barrier\n"
    "s_add_u32 m0, s74, 114688\n"
    "s_add_u32 s65, s96, s79\n"
    "buffer_load_dwordx4 v18, s[32:35], s65 offen lds\n"
    "s_add_u32 m0, s74, 115712\n"
    "s_add_u32 s65, s97, s79\n"
    "buffer_load_dwordx4 v18, s[32:35], s65 offen lds\n"
    "s_add_u32 m0, s74, 116736\n"
    "s_add_u32 s65, s98, s79\n"
    "buffer_load_dwordx4 v18, s[32:35], s65 offen lds\n"
    "s_add_u32 m0, s74, 117760\n"
    "s_add_u32 s65, s99, s79\n"
    "buffer_load_dwordx4 v18, s[32:35], s65 offen lds\n"
    "s_add_u32 s78, s88, 3\n"
    "s_cmp_ge_u32 s78, 7\n"
    "s_cselect_b32 s79, 7, 0\n"
    "s_sub_u32 s78, s78, s79\n"
    "s_add_u32 s78, s78, 1\n"
    "s_lshl_b32 s79, s78, 19\n"
    "s_lshl_b32 s78, s78, 7\n"
    "s_add_u32 m0, s74, 131072\n"
    "s_add_u32 s66, s70, s78\n"
    "buffer_load_dwordx4 v16, s[20:23], s66 offen lds\n"
    "s_add_u32 m0, s74, 132096\n"
    "s_add_u32 s66, s71, s78\n"
    "buffer_load_dwordx4 v17, s[20:23], s66 offen lds\n"
    "s_add_u32 m0, s74, 133120\n"
    "s_add_u32 s66, s72, s78\n"
    "buffer_load_dwordx4 v16, s[20:23], s66 offen lds\n"
    "s_add_u32 m0, s74, 134144\n"
    "s_add_u32 s66, s73, s78\n"
    "buffer_load_dwordx4 v17, s[20:23], s66 offen lds\n"
    "s_waitcnt vmcnt(12)\n"
    "s_barrier\n"
    "s_add_u32 m0, s74, 147456\n"
    "s_add_u32 s65, s96, s79\n"
    "buffer_load_dwordx4 v18, s[32:35], s65 offen lds\n"
    "s_add_u32 m0, s74, 148480\n"
    "s_add_u32 s65, s97, s79\n"
    "buffer_load_dwordx4 v18, s[32:35], s65 offen lds\n"
    "s_add_u32 m0, s74, 149504\n"
    "s_add_u32 s65, s98, s79\n"
    "buffer_load_dwordx4 v18, s[32:35], s65 offen lds\n"
    "s_add_u32 m0, s74, 150528\n"
    "s_add_u32 s65, s99, s79\n"
    "buffer_load_dwordx4 v18, s[32:35], s65 offen lds\n"
    "s_add_u32 s78, s88, 4\n"
    "s_cmp_ge_u32 s78, 7\n"
    "s_cselect_b32 s79, 7, 0\n"
    "s_sub_u32 s78, s78, s79\n"
    "s_add_u32 s78, s78, 1\n"
    "s_lshl_b32 s79, s78, 19\n"
    "s_lshl_b32 s78, s78, 7\n"
    "s_add_u32 m0, s74, 0\n"
    "s_add_u32 s66, s70, s78\n"
    "buffer_load_dwordx4 v16, s[20:23], s66 offen lds\n"
    "s_add_u32 m0, s74, 16384\n"
    "s_add_u32 s65, s96, s79\n"
    "buffer_load_dwordx4 v18, s[32:35], s65 offen lds\n"
    "s_add_u32 m0, s74, 1024\n"
    "s_add_u32 s66, s71, s78\n"
    "buffer_load_dwordx4 v17, s[20:23], s66 offen lds\n"
    "s_add_u32 m0, s74, 17408\n"
    "s_add_u32 s65, s97, s79\n"
    "buffer_load_dwordx4 v18, s[32:35], s65 offen lds\n"
    "s_add_u32 m0, s74, 2048\n"
    "s_add_u32 s66, s72, s78\n"
    "buffer_load_dwordx4 v16, s[20:23], s66 offen lds\n"
    "s_add_u32 m0, s74, 18432\n"
    "s_add_u32 s65, s98, s79\n"
    "buffer_load_dwordx4 v18, s[32:35], s65 offen lds\n"
    "s_add_u32 m0, s74, 3072\n"
    "s_add_u32 s66, s73, s78\n"
    "buffer_load_dwordx4 v17, s[20:23], s66 offen lds\n"
    "s_add_u32 m0, s74, 19456\n"
    "s_add_u32 s65, s99, s79\n"
    "buffer_load_dwordx4 v18, s[32:35], s65 offen lds\n"
    "s_waitcnt vmcnt(16)\n"
    "s_barrier\n"
    "s_add_u32 s78, s88, 5\n"
    "s_cmp_ge_u32 s78, 7\n"
    "s_cselect_b32 s79, 7, 0\n"
    "s_sub_u32 s78, s78, s79\n"
    "s_add_u32 s78, s78, 1\n"
    "s_lshl_b32 s79, s78, 19\n"
    "s_lshl_b32 s78, s78, 7\n"
    "s_add_u32 m0, s74, 32768\n"
    "s_add_u32 s66, s70, s78\n"
    "buffer_load_dwordx4 v16, s[20:23], s66 offen lds\n"
    "s_add_u32 m0, s74, 49152\n"
    "s_add_u32 s65, s96, s79\n"
    "buffer_load_dwordx4 v18, s[32:35], s65 offen lds\n"
    "s_add_u32 m0, s74, 33792\n"
    "s_add_u32 s66, s71, s78\n"
    "buffer_load_dwordx4 v17, s[20:23], s66 offen lds\n"
    "s_add_u32 m0, s74, 50176\n"
    "s_add_u32 s65, s97, s79\n"
    "buffer_load_dwordx4 v18, s[32:35], s65 offen lds\n"
    "s_add_u32 m0, s74, 34816\n"
    "s_add_u32 s66, s72, s78\n"
    "buffer_load_dwordx4 v16, s[20:23], s66 offen lds\n"
    "s_add_u32 m0, s74, 51200\n"
    "s_add_u32 s65, s98, s79\n"
    "buffer_load_dwordx4 v18, s[32:35], s65 offen lds\n"
    "s_add_u32 m0, s74, 35840\n"
    "s_add_u32 s66, s73, s78\n"
    "buffer_load_dwordx4 v17, s[20:23], s66 offen lds\n"
    "s_add_u32 m0, s74, 52224\n"
    "s_add_u32 s65, s99, s79\n"
    "buffer_load_dwordx4 v18, s[32:35], s65 offen lds\n"
    "buffer_load_dword v14, v3, s[36:39], 0 offen nt\n"
    "s_waitcnt vmcnt(17)\n"
    "s_barrier\n"
    "s_add_u32 s78, s88, 6\n"
    "s_cmp_ge_u32 s78, 7\n"
    "s_cselect_b32 s79, 7, 0\n"
    "s_sub_u32 s78, s78, s79\n"
    "s_add_u32 s78, s78, 1\n"
    "s_lshl_b32 s79, s78, 19\n"
    "s_lshl_b32 s78, s78, 7\n"
    "s_add_u32 m0, s74, 65536\n"
    "s_add_u32 s66, s70, s78\n"
    "buffer_load_dwordx4 v16, s[20:23], s66 offen lds\n"
    "s_add_u32 m0, s74, 81920\n"
    "s_add_u32 s65, s96, s79\n"
    "buffer_load_dwordx4 v18, s[32:35], s65 offen lds\n"
    "s_add_u32 m0, s74, 66560\n"
    "s_add_u32 s66, s71, s78\n"
    "buffer_load_dwordx4 v17, s[20:23], s66 offen lds\n"
    "s_add_u32 m0, s74, 82944\n"
    "s_add_u32 s65, s97, s79\n"
    "buffer_load_dwordx4 v18, s[32:35], s65 offen lds\n"
    "s_add_u32 m0, s74, 67584\n"
    "s_add_u32 s66, s72, s78\n"
    "buffer_load_dwordx4 v16, s[20:23], s66 offen lds\n"
    "s_add_u32 m0, s74, 83968\n"
    "s_add_u32 s65, s98, s79\n"
    "buffer_load_dwordx4 v18, s[32:35], s65 offen lds\n"
    "s_add_u32 m0, s74, 68608\n"
    "s_add_u32 s66, s73, s78\n"
    "buffer_load_dwordx4 v17, s[20:23], s66 offen lds\n"
    "s_add_u32 m0, s74, 84992\n"
    "s_add_u32 s65, s99, s79\n"
    "buffer_load_dwordx4 v18, s[32:35], s65 offen lds\n"
    "s_waitcnt vmcnt(17)\n"
    "s_barrier\n"
    "s_lshl_b32 s65, s14, 10\n"
    "s_add_u32 s66, s65, 0\n"
    "s_add_u32 m0, s66, 98304\n"
    "s_nop 0\n"
    "buffer_load_dwordx4 v12, s[24:27], s66 offen nt lds\n"
    "s_add_u32 s66, s65, 4096\n"
    "s_add_u32 m0, s66, 98304\n"
    "s_nop 0\n"
    "buffer_load_dwordx4 v12, s[24:27], s66 offen nt lds\n"
    "s_add_u32 s66, s65, 8192\n"
    "s_add_u32 m0, s66, 98304\n"
    "s_nop 0\n"
    "buffer_load_dwordx4 v12, s[24:27], s66 offen nt lds\n"
    "s_add_u32 s66, s65, 12288\n"
    "s_add_u32 m0, s66, 98304\n"
    "s_nop 0\n"
    "buffer_load_dwordx4 v12, s[24:27], s66 offen nt lds\n"
    "s_add_u32 s66, s65, 16384\n"
    "s_add_u32 m0, s66, 98304\n"
    "s_nop 0\n"
    "buffer_load_dwordx4 v12, s[24:27], s66 offen nt lds\n"
    "s_add_u32 s66, s65, 20480\n"
    "s_add_u32 m0, s66, 98304\n"
    "s_nop 0\n"
    "buffer_load_dwordx4 v12, s[24:27], s66 offen nt lds\n"
    "s_add_u32 s66, s65, 24576\n"
    "s_add_u32 m0, s66, 98304\n"
    "s_nop 0\n"
    "buffer_load_dwordx4 v12, s[24:27], s66 offen nt lds\n"
    "s_add_u32 s66, s65, 28672\n"
    "s_add_u32 m0, s66, 98304\n"
    "s_nop 0\n"
    "buffer_load_dwordx4 v12, s[24:27], s66 offen nt lds\n"
    "s_waitcnt vmcnt(16)\n"
    "buffer_load_dword v14, v3, s[36:39], 0 offen\n"
    "s_barrier\n"
    "s_waitcnt vmcnt(9)\n"
    "s_barrier\n"
    "s_lshl_b32 s65, s14, 13\n"
    "s_add_u32 s66, s65, 0\n"
    "s_add_u32 m0, s66, 131072\n"
    "s_nop 0\n"
    "buffer_load_dwordx4 v13, s[28:31], s66 offen nt lds\n"
    "s_add_u32 s66, s65, 1024\n"
    "s_add_u32 m0, s66, 131072\n"
    "s_nop 0\n"
    "buffer_load_dwordx4 v13, s[28:31], s66 offen nt lds\n"
    "s_add_u32 s66, s65, 2048\n"
    "s_add_u32 m0, s66, 131072\n"
    "s_nop 0\n"
    "buffer_load_dwordx4 v13, s[28:31], s66 offen nt lds\n"
    "s_add_u32 s66, s65, 3072\n"
    "s_add_u32 m0, s66, 131072\n"
    "s_nop 0\n"
    "buffer_load_dwordx4 v13, s[28:31], s66 offen nt lds\n"
    "s_add_u32 s66, s65, 4096\n"
    "s_add_u32 m0, s66, 131072\n"
    "s_nop 0\n"
    "buffer_load_dwordx4 v13, s[28:31], s66 offen nt lds\n"
    "s_add_u32 s66, s65, 5120\n"
    "s_add_u32 m0, s66, 131072\n"
    "s_nop 0\n"
    "buffer_load_dwordx4 v13, s[28:31], s66 offen nt lds\n"
    "s_add_u32 s66, s65, 6144\n"
    "s_add_u32 m0, s66, 131072\n"
    "s_nop 0\n"
    "buffer_load_dwordx4 v13, s[28:31], s66 offen nt lds\n"
    "s_add_u32 s66, s65, 7168\n"
    "s_add_u32 m0, s66, 131072\n"
    "s_nop 0\n"
    "buffer_load_dwordx4 v13, s[28:31], s66 offen nt lds\n"
    "s_waitcnt vmcnt(9)\n"
    "s_barrier\n"
    "s_add_u32 s66, s12, 0\n"
    "s_and_b32 s66, s66, 3\n"
    "s_lshl_b32 s66, s66, 17\n"
    "s_lshl_b32 s67, s14, 15\n"
    "s_add_u32 s66, s66, s67\n"
    "s_lshl_b32 s67, s14, 13\n"
    "s_add_u32 m0, s67, 0\n"
    "s_add_u32 s65, s66, 0\n"
    "buffer_load_dwordx4 v19, s[36:39], s65 offen lds\n"
    "s_add_u32 m0, s67, 1024\n"
    "s_add_u32 s65, s66, 4096\n"
    "buffer_load_dwordx4 v19, s[36:39], s65 offen lds\n"
    "s_add_u32 m0, s67, 2048\n"
    "s_add_u32 s65, s66, 8192\n"
    "buffer_load_dwordx4 v19, s[36:39], s65 offen lds\n"
    "s_add_u32 m0, s67, 3072\n"
    "s_add_u32 s65, s66, 12288\n"
    "buffer_load_dwordx4 v19, s[36:39], s65 offen lds\n"
    "s_add_u32 m0, s67, 4096\n"
    "s_add_u32 s65, s66, 16384\n"
    "buffer_load_dwordx4 v19, s[36:39], s65 offen lds\n"
    "s_add_u32 m0, s67, 5120\n"
    "s_add_u32 s65, s66, 20480\n"
    "buffer_load_dwordx4 v19, s[36:39], s65 offen lds\n"
    "s_add_u32 m0, s67, 6144\n"
    "s_add_u32 s65, s66, 24576\n"
    "buffer_load_dwordx4 v19, s[36:39], s65 offen lds\n"
    "s_add_u32 m0, s67, 7168\n"
    "s_add_u32 s65, s66, 28672\n"
    "buffer_load_dwordx4 v19, s[36:39], s65 offen lds\n"
    "s_add_u32 s66, s12, 1\n"
    "s_and_b32 s66, s66, 3\n"
    "s_lshl_b32 s66, s66, 17\n"
    "s_lshl_b32 s67, s14, 15\n"
    "s_add_u32 s66, s66, s67\n"
    "s_lshl_b32 s67, s14, 13\n"
    "s_add_u32 m0, s67, 32768\n"
    "s_add_u32 s65, s66, 0\n"
    "buffer_load_dwordx4 v19, s[36:39], s65 offen lds\n"
    "s_add_u32 m0, s67, 33792\n"
    "s_add_u32 s65, s66, 4096\n"
    "buffer_load_dwordx4 v19, s[36:39], s65 offen lds\n"
    "s_add_u32 m0, s67, 34816\n"
    "s_add_u32 s65, s66, 8192\n"
    "buffer_load_dwordx4 v19, s[36:39], s65 offen lds\n"
    "s_add_u32 m0, s67, 35840\n"
    "s_add_u32 s65, s66, 12288\n"
    "buffer_load_dwordx4 v19, s[36:39], s65 offen lds\n"
    "s_add_u32 m0, s67, 36864\n"
    "s_add_u32 s65, s66, 16384\n"
    "buffer_load_dwordx4 v19, s[36:39], s65 offen lds\n"
    "s_add_u32 m0, s67, 37888\n"
    "s_add_u32 s65, s66, 20480\n"
    "buffer_load_dwordx4 v19, s[36:39], s65 offen lds\n"
    "s_add_u32 m0, s67, 38912\n"
    "s_add_u32 s65, s66, 24576\n"
    "buffer_load_dwordx4 v19, s[36:39], s65 offen lds\n"
    "s_add_u32 m0, s67, 39936\n"
    "s_add_u32 s65, s66, 28672\n"
    "buffer_load_dwordx4 v19, s[36:39], s65 offen lds\n"
    "s_waitcnt vmcnt(16)\n"
    "s_barrier\n"
    "s_add_u32 s66, s12, 2\n"
    "s_and_b32 s66, s66, 3\n"
    "s_lshl_b32 s66, s66, 17\n"
    "s_lshl_b32 s67, s14, 15\n"
    "s_add_u32 s66, s66, s67\n"
    "s_lshl_b32 s67, s14, 13\n"
    "s_add_u32 m0, s67, 65536\n"
    "s_add_u32 s65, s66, 0\n"
    "buffer_load_dwordx4 v19, s[36:39], s65 offen lds\n"
    "s_add_u32 m0, s67, 66560\n"
    "s_add_u32 s65, s66, 4096\n"
    "buffer_load_dwordx4 v19, s[36:39], s65 offen lds\n"
    "s_add_u32 m0, s67, 67584\n"
    "s_add_u32 s65, s66, 8192\n"
    "buffer_load_dwordx4 v19, s[36:39], s65 offen lds\n"
    "s_add_u32 m0, s67, 68608\n"
    "s_add_u32 s65, s66, 12288\n"
    "buffer_load_dwordx4 v19, s[36:39], s65 offen lds\n"
    "s_add_u32 m0, s67, 69632\n"
    "s_add_u32 s65, s66, 16384\n"
    "buffer_load_dwordx4 v19, s[36:39], s65 offen lds\n"
    "s_add_u32 m0, s67, 70656\n"
    "s_add_u32 s65, s66, 20480\n"
    "buffer_load_dwordx4 v19, s[36:39], s65 offen lds\n"
    "s_add_u32 m0, s67, 71680\n"
    "s_add_u32 s65, s66, 24576\n"
    "buffer_load_dwordx4 v19, s[36:39], s65 offen lds\n"
    "s_add_u32 m0, s67, 72704\n"
    "s_add_u32 s65, s66, 28672\n"
    "buffer_load_dwordx4 v19, s[36:39], s65 offen lds\n"
    "s_add_u32 s66, s12, 3\n"
    "s_and_b32 s66, s66, 3\n"
    "s_lshl_b32 s66, s66, 17\n"
    "s_lshl_b32 s67, s14, 15\n"
    "s_add_u32 s66, s66, s67\n"
    "s_lshl_b32 s67, s14, 13\n"
    "s_add_u32 m0, s67, 98304\n"
    "s_add_u32 s65, s66, 0\n"
    "buffer_load_dwordx4 v19, s[36:39], s65 offen lds\n"
    "s_add_u32 m0, s67, 99328\n"
    "s_add_u32 s65, s66, 4096\n"
    "buffer_load_dwordx4 v19, s[36:39], s65 offen lds\n"
    "s_add_u32 m0, s67, 100352\n"
    "s_add_u32 s65, s66, 8192\n"
    "buffer_load_dwordx4 v19, s[36:39], s65 offen lds\n"
    "s_add_u32 m0, s67, 101376\n"
    "s_add_u32 s65, s66, 12288\n"
    "buffer_load_dwordx4 v19, s[36:39], s65 offen lds\n"
    "s_add_u32 m0, s67, 102400\n"
    "s_add_u32 s65, s66, 16384\n"
    "buffer_load_dwordx4 v19, s[36:39], s65 offen lds\n"
    "s_add_u32 m0, s67, 103424\n"
    "s_add_u32 s65, s66, 20480\n"
    "buffer_load_dwordx4 v19, s[36:39], s65 offen lds\n"
    "s_add_u32 m0, s67, 104448\n"
    "s_add_u32 s65, s66, 24576\n"
    "buffer_load_dwordx4 v19, s[36:39], s65 offen lds\n"
    "s_add_u32 m0, s67, 105472\n"
    "s_add_u32 s65, s66, 28672\n"
    "buffer_load_dwordx4 v19, s[36:39], s65 offen lds\n"
    "s_waitcnt vmcnt(24)\n"
    "s_barrier\n"
    "s_waitcnt vmcnt(16)\n"
    "s_waitcnt vmcnt(8)\n"
    "s_waitcnt vmcnt(0)\n"
    "s_barrier\n"
    "s_barrier\n"
    "s_sleep 16\n"
    "buffer_load_dwordx2 v[4:5], v20, s[44:47], 0 offen sc1\n"
    "s_waitcnt lgkmcnt(0)\n"
    "s_mov_b32 s93, 262144\n"
    "L_p2_1_%=:\n"
    "s_waitcnt vmcnt(0)\n"
    "v_cmp_eq_u64 vcc, v[4:5], s[62:63]\n"
    "s_nop 1\n"
    "s_and_b64 vcc, vcc, 255\n"
    "s_cmp_eq_u64 vcc, 255\n"
    "s_cbranch_scc1 L_p2d_2_%=\n"
    "s_andn2_b64 exec, 255, vcc\n"
    "buffer_load_dwordx2 v[4:5], v20, s[44:47], 0 offen sc1\n"
    "s_mov_b64 exec, -1\n"
    "s_sub_u32 s93, s93, 1\n"
    "s_cmp_lg_u32 s93, 0\n"
    "s_cbranch_scc1 L_p2_1_%=\n"
    "s_waitcnt vmcnt(0)\n"
    "L_p2d_2_%=:\n"
    "; @reset\n"
    "s_add_u32 s66, s80, 0\n"
    "buffer_load_dwordx4 v[64:67], v22, s[44:47], s66 offen sc1\n"
    "s_add_u32 s66, s80, 32768\n"
    "buffer_load_dwordx4 v[68:71], v22, s[44:47], s66 offen sc1\n"
    "s_add_u32 s66, s80, 65536\n"
    "buffer_load_dwordx4 v[72:75], v22, s[44:47], s66 offen sc1\n"
    "s_add_u32 s66, s80, 98304\n"
    "buffer_load_dwordx4 v[76:79], v22, s[44:47], s66 offen sc1\n"
    "s_add_u32 s66, s80, 131072\n"
    "buffer_load_dwordx4 v[80:83], v22, s[44:47], s66 offen sc1\n"
    "s_add_u32 s66, s80, 163840\n"
    "buffer_load_dwordx4 v[84:87], v22, s[44:47], s66 offen sc1\n"
    "s_add_u32 s66, s80, 196608\n"
    "buffer_load_dwordx4 v[88:91], v22, s[44:47], s66 offen sc1\n"
    "s_add_u32 s66, s80, 229376\n"
    "buffer_load_dwordx4 v[92:95], v22, s[44:47], s66 offen sc1\n"
    "s_waitcnt vmcnt(7)\n"
    "v_lshlrev_b32 v100, 16, v64\n"
    "v_and_b32 v101, 0xffff0000, v64\n"
    "v_lshlrev_b32 v102, 16, v65\n"
    "v_and_b32 v103, 0xffff0000, v65\n"
    "v_lshlrev_b32 v104, 16, v66\n"
    "v_and_b32 v105, 0xffff0000, v66\n"
    "v_lshlrev_b32 v106, 16, v67\n"
    "v_and_b32 v107, 0xffff0000, v67\n"
    "s_waitcnt vmcnt(6)\n"
    "v_lshlrev_b32 v6, 16, v68\n"
    "v_and_b32 v7, 0xffff0000, v68\n"
    "v_add_f32 v100, v100, v6\n"
    "v_add_f32 v101, v101, v7\n"
    "v_lshlrev_b32 v8, 16, v69\n"
    "v_and_b32 v9, 0xffff0000, v69\n"
    "v_add_f32 v102, v102, v8\n"
    "v_add_f32 v103, v103, v9\n"
    "v_lshlrev_b32 v6, 16, v70\n"
    "v_and_b32 v7, 0xffff0000, v70\n"
    "v_add_f32 v104, v104, v6\n"
    "v_add_f32 v105, v105, v7\n"
    "v_lshlrev_b32 v8, 16, v71\n"
    "v_and_b32 v9, 0xffff0000, v71\n"
    "v_add_f32 v106, v106, v8\n"
    "v_add_f32 v107, v107, v9\n"
    "s_waitcnt vmcnt(5)\n"
    "v_lshlrev_b32 v6, 16, v72\n"
    "v_and_b32 v7, 0xffff0000, v72\n"
    "v_add_f32 v100, v100, v6\n"
    "v_add_f32 v101, v101, v7\n"
    "v_lshlrev_b32 v8, 16, v73\n"
    "v_and_b32 v9, 0xffff0000, v73\n"
    "v_add_f32 v102, v102, v8\n"
    "v_add_f32 v103, v103, v9\n"
    "v_lshlrev_b32 v6, 16, v74\n"
    "v_and_b32 v7, 0xffff0000, v74\n"
    "v_add_f32 v104, v104, v6\n"
    "v_add_f32 v105, v105, v7\n"
    "v_lshlrev_b32 v8, 16, v75\n"
    "v_and_b32 v9, 0xffff0000, v75\n"
    "v_add_f32 v106, v106, v8\n"
    "v_add_f32 v107, v107, v9\n"
    "s_waitcnt vmcnt(4)\n"
    "v_lshlrev_b32 v6, 16, v76\n"
    "v_and_b32 v7, 0xffff0000, v76\n"
    "v_add_f32 v100, v100, v6\n"
    "v_add_f32 v101, v101, v7\n"
    "v_lshlrev_b32 v8, 16, v77\n"
    "v_and_b32 v9, 0xffff0000, v77\n"
    "v_add_f32 v102, v102, v8\n"
    "v_add_f32 v103, v103, v9\n"
    "v_lshlrev_b32 v6, 16, v78\n"
    "v_and_b32 v7, 0xffff0000, v78\n"
    "v_add_f32 v104, v104, v6\n"
    "v_add_f32 v105, v105, v7\n"
    "v_lshlrev_b32 v8, 16, v79\n"
    "v_and_b32 v9, 0xffff0000, v79\n"
    "v_add_f32 v106, v106, v8\n"
    "v_add_f32 v107, v107, v9\n"
    "s_waitcnt vmcnt(3)\n"
    "v_lshlrev_b32 v6, 16, v80\n"
    "v_and_b32 v7, 0xffff0000, v80\n"
    "v_add_f32 v100, v100, v6\n"
    "v_add_f32 v101, v101, v7\n"
    "v_lshlrev_b32 v8, 16, v81\n"
    "v_and_b32 v9, 0xffff0000, v81\n"
    "v_add_f32 v102, v102, v8\n"
    "v_add_f32 v103, v103, v9\n"
    "v_lshlrev_b32 v6, 16, v82\n"
    "v_and_b32 v7, 0xffff0000, v82\n"
    "v_add_f32 v104, v104, v6\n"
    "v_add_f32 v105, v105, v7\n"
    "v_lshlrev_b32 v8, 16, v83\n"
    "v_and_b32 v9, 0xffff0000, v83\n"
    "v_add_f32 v106, v106, v8\n"
    "v_add_f32 v107, v107, v9\n"
    "s_waitcnt vmcnt(2)\n"
    "v_lshlrev_b32 v6, 16, v84\n"
    "v_and_b32 v7, 0xffff0000, v84\n"
    "v_add_f32 v100, v100, v6\n"
    "v_add_f32 v101, v101, v7\n"
    "v_lshlrev_b32 v8, 16, v85\n"
    "v_and_b32 v9, 0xffff0000, v85\n"
    "v_add_f32 v102, v102, v8\n"
    "v_add_f32 v103, v103, v9\n"
    "v_lshlrev_b32 v6, 16, v86\n"
    "v_and_b32 v7, 0xffff0000, v86\n"
    "v_add_f32 v104, v104, v6\n"
    "v_add_f32 v105, v105, v7\n"
    "v_lshlrev_b32 v8, 16, v87\n"
    "v_and_b32 v9, 0xffff0000, v87\n"
    "v_add_f32 v106, v106, v8\n"
    "v_add_f32 v107, v107, v9\n"
    "s_waitcnt vmcnt(1)\n"
    "v_lshlrev_b32 v6, 16, v88\n"
    "v_and_b32 v7, 0xffff0000, v88\n"
    "v_add_f32 v100, v100, v6\n"
    "v_add_f32 v101, v101, v7\n"
    "v_lshlrev_b32 v8, 16, v89\n"
    "v_and_b32 v9, 0xffff0000, v89\n"
    "v_add_f32 v102, v102, v8\n"
    "v_add_f32 v103, v103, v9\n"
    "v_lshlrev_b32 v6, 16, v90\n"
    "v_and_b32 v7, 0xffff0000, v90\n"
    "v_add_f32 v104, v104, v6\n"
    "v_add_f32 v105, v105, v7\n"
    "v_lshlrev_b32 v8, 16, v91\n"
    "v_and_b32 v9, 0xffff0000, v91\n"
    "v_add_f32 v106, v106, v8\n"
    "v_add_f32 v107, v107, v9\n"
    "s_waitcnt vmcnt(0)\n"
    "v_lshlrev_b32 v6, 16, v92\n"
    "v_and_b32 v7, 0xffff0000, v92\n"
    "v_add_f32 v100, v100, v6\n"
    "v_add_f32 v101, v101, v7\n"
    "v_lshlrev_b32 v8, 16, v93\n"
    "v_and_b32 v9, 0xffff0000, v93\n"
    "v_add_f32 v102, v102, v8\n"
    "v_add_f32 v103, v103, v9\n"
    "v_lshlrev_b32 v6, 16, v94\n"
    "v_and_b32 v7, 0xffff0000, v94\n"
    "v_add_f32 v104, v104, v6\n"
    "v_add_f32 v105, v105, v7\n"
    "v_lshlrev_b32 v8, 16, v95\n"
    "v_and_b32 v9, 0xffff0000, v95\n"
    "v_add_f32 v106, v106, v8\n"
    "v_add_f32 v107, v107, v9\n"
    "v_cvt_pk_bf16_f32 v64, v100, v101\n"
    "v_cvt_pk_bf16_f32 v65, v102, v103\n"
    "v_cvt_pk_bf16_f32 v66, v104, v105\n"
    "v_cvt_pk_bf16_f32 v67, v106, v107\n"
    "s_nop 0\n"
    "v_permlane32_swap_b32 v64, v66\n"
    "v_permlane32_swap_b32 v65, v67\n"
    "buffer_store_dwordx4 v[64:67], v21, s[40:43], s81 offen sc1\n"
    "s_and_b32 s66, s9, 31\n"
    "s_xor_b32 s67, s14, 3\n"
    "s_or_b32 s66, s66, s67\n"
    "s_cmp_lg_u32 s66, 0\n"
    "s_cbranch_scc1 L_bend_3_%=\n"
    "s_lshl_b32 s66, s10, 7\n"
    "v_mov_b32 v4, s62\n"
    "v_mov_b32 v5, s63\n"
    "v_mov_b32 v6, 0\n"
    "s_mov_b64 exec, 1\n"
    "buffer_store_dwordx2 v[4:5], v6, s[44:47], s66 offen sc1\n"
    "s_mov_b64 exec, -1\n"
    "L_bend_3_%=:\n"
    "s_endpgm\n"
    "L_pathA_%=:\n"
    "; @reset\n"
    "v_and_b32 v1, 31, v0\n"
    "v_lshrrev_b32 v2, 5, v0\n"
    "v_lshrrev_b32 v4, 1, v1\n"
    "v_and_b32 v4, 7, v4\n"
    "s_lshl_b32 s15, s14, 5\n"
    "v_add_u32 v5, s15, v1\n"
    "v_lshlrev_b32 v5, 7, v5\n"
    "v_add_u32 v6, 0, v2\n"
    "v_xor_b32 v6, v6, v4\n"
    "v_lshl_add_u32 v16, v6, 4, v5\n"
    "v_add_u32 v20, 65536, v16\n"
    "v_add_u32 v24, 131072, v16\n"
    "v_add_u32 v6, 2, v2\n"
    "v_xor_b32 v6, v6, v4\n"
    "v_lshl_add_u32 v17, v6, 4, v5\n"
    "v_add_u32 v21, 65536, v17\n"
    "v_add_u32 v25, 131072, v17\n"
    "v_add_u32 v6, 4, v2\n"
    "v_xor_b32 v6, v6, v4\n"
    "v_lshl_add_u32 v18, v6, 4, v5\n"
    "v_add_u32 v22, 65536, v18\n"
    "v_add_u32 v26, 131072, v18\n"
    "v_add_u32 v6, 6, v2\n"
    "v_xor_b32 v6, v6, v4\n"
    "v_lshl_add_u32 v19, v6, 4, v5\n"
    "v_add_u32 v23, 65536, v19\n"
    "v_add_u32 v27, 131072, v19\n"
    "v_bfe_u32 v4, v0, 2, 2\n"
    "v_bfe_u32 v5, v0, 4, 1\n"
    "v_bfe_u32 v6, v0, 1, 1\n"
    "v_lshl_add_u32 v5, v5, 1, v6\n"
    "v_and_b32 v8, 1, v0\n"
    "v_lshl_add_u32 v7, v2, 3, v4\n"
    "v_lshlrev_b32 v7, 8, v7\n"
    "v_lshl_add_u32 v7, v8, 3, v7\n"
    "v_add_u32 v7, 16384, v7\n"
    "v_xor_b32 v6, 0, v4\n"
    "v_lshl_add_u32 v6, v6, 2, v5\n"
    "v_lshl_add_u32 v28, v6, 4, v7\n"
    "v_add_u32 v32, 65536, v28\n"
    "v_add_u32 v36, 131072, v28\n"
    "v_xor_b32 v6, 1, v4\n"
    "v_lshl_add_u32 v6, v6, 2, v5\n"
    "v_lshl_add_u32 v29, v6, 4, v7\n"
    "v_add_u32 v33, 65536, v29\n"
    "v_add_u32 v37, 131072, v29\n"
    "v_xor_b32 v6, 2, v4\n"
    "v_lshl_add_u32 v6, v6, 2, v5\n"
    "v_lshl_add_u32 v30, v6, 4, v7\n"
    "v_add_u32 v34, 65536, v30\n"
    "v_add_u32 v38, 131072, v30\n"
    "v_xor_b32 v6, 3, v4\n"
    "v_lshl_add_u32 v6, v6, 2, v5\n"
    "v_lshl_add_u32 v31, v6, 4, v7\n"
    "v_add_u32 v35, 65536, v31\n"
    "v_add_u32 v39, 131072, v31\n"
    "v_lshl_add_u32 v7, v2, 2, v4\n"
    "v_lshlrev_b32 v7, 8, v7\n"
    "v_lshl_add_u32 v7, v8, 3, v7\n"
    "v_xor_b32 v6, 0, v4\n"
    "v_lshl_add_u32 v6, v6, 2, v5\n"
    "v_lshl_add_u32 v48, v6, 4, v7\n"
    "v_add_u32 v52, 65536, v48\n"
    "v_add_u32 v56, 131072, v48\n"
    "v_xor_b32 v6, 1, v4\n"
    "v_lshl_add_u32 v6, v6, 2, v5\n"
    "v_lshl_add_u32 v49, v6, 4, v7\n"
    "v_add_u32 v53, 65536, v49\n"
    "v_add_u32 v57, 131072, v49\n"
    "v_xor_b32 v6, 2, v4\n"
    "v_lshl_add_u32 v6, v6, 2, v5\n"
    "v_lshl_add_u32 v50, v6, 4, v7\n"
    "v_add_u32 v54, 65536, v50\n"
    "v_add_u32 v58, 131072, v50\n"
    "v_xor_b32 v6, 3, v4\n"
    "v_lshl_add_u32 v6, v6, 2, v5\n"
    "v_lshl_add_u32 v51, v6, 4, v7\n"
    "v_add_u32 v55, 65536, v51\n"
    "v_add_u32 v59, 131072, v51\n"
    "v_and_b32 v4, 15, v1\n"
    "v_xor_b32 v4, v4, v2\n"
    "v_lshlrev_b32 v4, 4, v4\n"
    "v_lshl_add_u32 v4, v1, 8, v4\n"
    "v_add_u32 v4, 98304, v4\n"
    "v_xor_b32 v40, 0, v4\n"
    "v_xor_b32 v41, 32, v4\n"
    "v_xor_b32 v42, 64, v4\n"
    "v_xor_b32 v43, 96, v4\n"
    "v_xor_b32 v44, 128, v4\n"
    "v_xor_b32 v45, 160, v4\n"
    "v_xor_b32 v46, 192, v4\n"
    "v_xor_b32 v47, 224, v4\n"
    "v_lshlrev_b32 v60, 4, v0\n"
    "s_lshl_b32 s65, s9, 11\n"
    "s_lshl_b32 s66, s14, 7\n"
    "s_add_u32 s65, s65, s66\n"
    "v_mov_b32 v5, 0x7ffff000\n"
    "v_cmp_eq_u32 vcc, 0, v0\n"
    "s_add_u32 s66, s65, 4096\n"
    "v_mov_b32 v4, s66\n"
    "v_cndmask_b32 v61, v5, v4, vcc\n"
    "s_add_u32 s66, s65, 528384\n"
    "v_mov_b32 v4, s66\n"
    "v_cndmask_b32 v63, v5, v4, vcc\n"
    "v_add_u32 v4, 1, v0\n"
    "v_add_u32 v4, s12, v4\n"
    "v_and_b32 v4, 3, v4\n"
    "v_lshlrev_b32 v4, 3, v4\n"
    "s_lshl_b32 s65, s10, 5\n"
    "s_add_u32 s65, s65, s11\n"
    "v_add_u32 v4, s65, v4\n"
    "v_lshlrev_b32 v4, 11, v4\n"
    "s_lshl_b32 s66, s14, 7\n"
    "s_add_u32 s66, s66, 4096\n"
    "v_add_u32 v4, s66, v4\n"
    "v_cmp_gt_u32 vcc, 3, v0\n"
    "s_nop 1\n"
    "v_cndmask_b32 v62, v5, v4, vcc\n"
    "s_lshl_b32 s67, s14, 13\n"
    "s_xor_b32 s76, s9, 0\n"
    "s_lshl_b32 s76, s76, 15\n"
    "s_add_u32 s76, s76, s67\n"
    "s_add_u32 s76, s76, 2097152\n"
    "s_add_u32 s77, s76, 4096\n"
    "s_add_u32 s70, s12, 1\n"
    "s_and_b32 s70, s70, 3\n"
    "s_lshl_b32 s70, s70, 3\n"
    "s_add_u32 s70, s70, s65\n"
    "s_lshl_b32 s70, s70, 15\n"
    "s_add_u32 s70, s70, s67\n"
    "s_add_u32 s70, s70, 2097152\n"
    "s_add_u32 s71, s70, 4096\n"
    "s_add_u32 s72, s12, 2\n"
    "s_and_b32 s72, s72, 3\n"
    "s_lshl_b32 s72, s72, 3\n"
    "s_add_u32 s72, s72, s65\n"
    "s_lshl_b32 s72, s72, 15\n"
    "s_add_u32 s72, s72, s67\n"
    "s_add_u32 s72, s72, 2097152\n"
    "s_add_u32 s73, s72, 4096\n"
    "s_add_u32 s74, s12, 3\n"
    "s_and_b32 s74, s74, 3\n"
    "s_lshl_b32 s74, s74, 3\n"
    "s_add_u32 s74, s74, s65\n"
    "s_lshl_b32 s74, s74, 15\n"
    "s_add_u32 s74, s74, s67\n"
    "s_add_u32 s74, s74, 2097152\n"
    "s_add_u32 s75, s74, 4096\n"
    "s_lshl_b32 s78, s9, 15\n"
    "s_add_u32 s78, s78, s67\n"
    "s_add_u32 s78, s78, 10485760\n"
    "s_add_u32 s79, s78, 4096\n"
    "s_waitcnt lgkmcnt(0)\n"
    "; @reset\n"
    "s_lshl_b32 s15, s10, 7\n"
    "s_add_u32 s16, s60, s15\n"
    "s_addc_u32 s17, s61, 0\n"
    "s_load_dwordx2 s[62:63], s[16:17], 0x0\n"
    "s_lshl_b32 s15, s10, 17\n"
    "s_add_u32 s20, s48, s15\n"
    "s_addc_u32 s21, s49, 0\n"
    "s_and_b32 s21, s21, 0xffff\n"
    "s_sub_u32 s22, 1048576, s15\n"
    "s_lshl_b32 s15, s13, 8\n"
    "s_add_u32 s32, s54, s15\n"
    "s_addc_u32 s33, s55, 0\n"
    "s_and_b32 s33, s33, 0xffff\n"
    "s_sub_u32 s34, 4194304, s15\n"
    "s_lshl_b32 s15, s10, 17\n"
    "s_add_u32 s40, s58, s15\n"
    "s_addc_u32 s41, s59, 0\n"
    "s_and_b32 s41, s41, 0xffff\n"
    "s_sub_u32 s42, 1048576, s15\n"
    "s_lshl_b32 s15, s10, 5\n"
    "s_add_u32 s15, s15, s13\n"
    "s_lshl_b32 s15, s15, 15\n"
    "s_add_u32 s24, s50, s15\n"
    "s_addc_u32 s25, s51, 0\n"
    "s_and_b32 s25, s25, 0xffff\n"
    "s_sub_u32 s26, 8388608, s15\n"
    "s_add_u32 s28, s52, s15\n"
    "s_addc_u32 s29, s53, 0\n"
    "s_and_b32 s29, s29, 0xffff\n"
    "s_sub_u32 s30, 8388608, s15\n"
    "s_lshl_b32 s15, s11, 19\n"
    "s_xor_b32 s65, s12, 0\n"
    "s_lshl_b32 s65, s65, 8\n"
    "s_add_u32 s15, s15, s65\n"
    "s_add_u32 s36, s56, s15\n"
    "s_addc_u32 s37, s57, 0\n"
    "s_and_b32 s37, s37, 0xffff\n"
    "s_sub_u32 s38, 4194304, s15\n"
    "s_mov_b32 s44, s60\n"
    "s_and_b32 s45, s61, 0xffff\n"
    "s_mov_b32 s46, 18874368\n"
    "s_waitcnt lgkmcnt(0)\n"
    "s_add_u32 s62, s62, 1\n"
    "s_addc_u32 s63, s63, 0\n"
    "v_mov_b32 v12, s62\n"
    "v_mov_b32 v13, s63\n"
    "s_cmp_lg_u32 s14, 0\n"
    "s_cbranch_scc1 L_warm_4_%=\n"
    "s_sleep 8\n"
    "buffer_load_dword v224, off, s[24:27], 0\n"
    "buffer_load_dword v225, off, s[28:31], 0\n"
    "buffer_load_dword v226, off, s[36:39], 0\n"
    "buffer_load_dword v227, off, s[44:47], s76 sc1\n"
    "buffer_load_dword v228, off, s[44:47], s78 sc1\n"
    "buffer_load_dword v229, v62, s[44:47], 0 offen sc1\n"
    "buffer_load_dword v230, off, s[40:43], 0\n"
    "L_warm_4_%=:\n"
    "; @reset\n"
    "s_barrier\n"
    "ds_read_b128 v[144:147], v16 offset:0\n"
    "ds_read_b64_tr_b16 v[128:129], v28 offset:0\n"
    "ds_read_b64_tr_b16 v[130:131], v28 offset:1024\n"
    "ds_read_b64_tr_b16 v[132:133], v29 offset:0\n"
    "ds_read_b64_tr_b16 v[134:135], v29 offset:1024\n"
    "ds_read_b64_tr_b16 v[136:137], v30 offset:0\n"
    "ds_read_b64_tr_b16 v[138:139], v30 offset:1024\n"
    "ds_read_b64_tr_b16 v[140:141], v31 offset:0\n"
    "ds_read_b64_tr_b16 v[142:143], v31 offset:1024\n"
    "ds_read_b128 v[164:167], v17 offset:0\n"
    "ds_read_b64_tr_b16 v[148:149], v28 offset:4096\n"
    "ds_read_b64_tr_b16 v[150:151], v28 offset:5120\n"
    "ds_read_b64_tr_b16 v[152:153], v29 offset:4096\n"
    "ds_read_b64_tr_b16 v[154:155], v29 offset:5120\n"
    "ds_read_b64_tr_b16 v[156:157], v30 offset:4096\n"
    "ds_read_b64_tr_b16 v[158:159], v30 offset:5120\n"
    "ds_read_b64_tr_b16 v[160:161], v31 offset:4096\n"
    "ds_read_b64_tr_b16 v[162:163], v31 offset:5120\n"
    "s_waitcnt lgkmcnt(15)\n"
    "v_mfma_f32_32x32x16_bf16 v[64:79], v[128:131], v[144:147], 0\n"
    "ds_read_b128 v[184:187], v18 offset:0\n"
    "ds_read_b64_tr_b16 v[168:169], v28 offset:8192\n"
    "ds_read_b64_tr_b16 v[170:171], v28 offset:9216\n"
    "s_waitcnt lgkmcnt(15)\n"
    "v_mfma_f32_32x32x16_bf16 v[80:95], v[132:135], v[144:147], 0\n"
    "ds_read_b64_tr_b16 v[172:173], v29 offset:8192\n"
    "ds_read_b64_tr_b16 v[174:175], v29 offset:9216\n"
    "s_waitcnt lgkmcnt(15)\n"
    "v_mfma_f32_32x32x16_bf16 v[96:111], v[136:139], v[144:147], 0\n"
    "ds_read_b64_tr_b16 v[176:177], v30 offset:8192\n"
    "ds_read_b64_tr_b16 v[178:179], v30 offset:9216\n"
    "s_waitcnt lgkmcnt(15)\n"
    "v_mfma_f32_32x32x16_bf16 v[112:127], v[140:143], v[144:147], 0\n"
    "ds_read_b64_tr_b16 v[180:181], v31 offset:8192\n"
    "ds_read_b64_tr_b16 v[182:183], v31 offset:9216\n"
    "s_waitcnt lgkmcnt(15)\n"
    "v_mfma_f32_32x32x16_bf16 v[64:79], v[148:151], v[164:167], v[64:79]\n"
    "ds_read_b128 v[144:147], v19 offset:0\n"
    "ds_read_b64_tr_b16 v[128:129], v28 offset:12288\n"
    "ds_read_b64_tr_b16 v[130:131], v28 offset:13312\n"
    "s_waitcnt lgkmcnt(15)\n"
    "v_mfma_f32_32x32x16_bf16 v[80:95], v[152:155], v[164:167], v[80:95]\n"
    "ds_read_b64_tr_b16 v[132:133], v29 offset:12288\n"
    "ds_read_b64_tr_b16 v[134:135], v29 offset:13312\n"
    "s_waitcnt lgkmcnt(15)\n"
    "v_mfma_f32_32x32x16_bf16 v[96:111], v[156:159], v[164:167], v[96:111]\n"
    "ds_read_b64_tr_b16 v[136:137], v30 offset:12288\n"
    "ds_read_b64_tr_b16 v[138:139], v30 offset:13312\n"
    "s_waitcnt lgkmcnt(15)\n"
    "v_mfma_f32_32x32x16_bf16 v[112:127], v[160:163], v[164:167], v[112:127]\n"
    "ds_read_b64_tr_b16 v[140:141], v31 offset:12288\n"
    "ds_read_b64_tr_b16 v[142:143], v31 offset:13312\n"
    "s_barrier\n"
    "s_waitcnt lgkmcnt(15)\n"
    "v_mfma_f32_32x32x16_bf16 v[64:79], v[168:171], v[184:187], v[64:79]\n"
    "ds_read_b128 v[164:167], v16 offset:32768\n"
    "ds_read_b64_tr_b16 v[148:149], v28 offset:32768\n"
    "ds_read_b64_tr_b16 v[150:151], v28 offset:33792\n"
    "s_waitcnt lgkmcnt(15)\n"
    "v_mfma_f32_32x32x16_bf16 v[80:95], v[172:175], v[184:187], v[80:95]\n"
    "ds_read_b64_tr_b16 v[152:153], v29 offset:32768\n"
    "ds_read_b64_tr_b16 v[154:155], v29 offset:33792\n"
    "s_waitcnt lgkmcnt(15)\n"
    "v_mfma_f32_32x32x16_bf16 v[96:111], v[176:179], v[184:187], v[96:111]\n"
    "ds_read_b64_tr_b16 v[156:157], v30 offset:32768\n"
    "ds_read_b64_tr_b16 v[158:159], v30 offset:33792\n"
    "s_waitcnt lgkmcnt(15)\n"
    "v_mfma_f32_32x32x16_bf16 v[112:127], v[180:183], v[184:187], v[112:127]\n"
    "ds_read_b64_tr_b16 v[160:161], v31 offset:32768\n"
    "ds_read_b64_tr_b16 v[162:163], v31 offset:33792\n"
    "s_waitcnt lgkmcnt(15)\n"
    "v_mfma_f32_32x32x16_bf16 v[64:79], v[128:131], v[144:147], v[64:79]\n"
    "ds_read_b128 v[184:187], v17 offset:32768\n"
    "ds_read_b64_tr_b16 v[168:169], v28 offset:36864\n"
    "ds_read_b64_tr_b16 v[170:171], v28 offset:37888\n"
    "s_waitcnt lgkmcnt(15)\n"
    "v_mfma_f32_32x32x16_bf16 v[80:95], v[132:135], v[144:147], v[80:95]\n"
    "ds_read_b64_tr_b16 v[172:173], v29 offset:36864\n"
    "ds_read_b64_tr_b16 v[174:175], v29 offset:37888\n"
    "s_waitcnt lgkmcnt(15)\n"
    "v_mfma_f32_32x32x16_bf16 v[96:111], v[136:139], v[144:147], v[96:111]\n"
    "ds_read_b64_tr_b16 v[176:177], v30 offset:36864\n"
    "ds_read_b64_tr_b16 v[178:179], v30 offset:37888\n"
    "s_waitcnt lgkmcnt(15)\n"
    "v_mfma_f32_32x32x16_bf16 v[112:127], v[140:143], v[144:147], v[112:127]\n"
    "ds_read_b64_tr_b16 v[180:181], v31 offset:36864\n"
    "ds_read_b64_tr_b16 v[182:183], v31 offset:37888\n"
    "s_waitcnt lgkmcnt(15)\n"
    "v_mfma_f32_32x32x16_bf16 v[64:79], v[148:151], v[164:167], v[64:79]\n"
    "ds_read_b128 v[144:147], v18 offset:32768\n"
    "ds_read_b64_tr_b16 v[128:129], v28 offset:40960\n"
    "ds_read_b64_tr_b16 v[130:131], v28 offset:41984\n"
    "s_waitcnt lgkmcnt(15)\n"
    "v_mfma_f32_32x32x16_bf16 v[80:95], v[152:155], v[164:167], v[80:95]\n"
    "ds_read_b64_tr_b16 v[132:133], v29 offset:40960\n"
    "ds_read_b64_tr_b16 v[134:135], v29 offset:41984\n"
    "s_waitcnt lgkmcnt(15)\n"
    "v_mfma_f32_32x32x16_bf16 v[96:111], v[156:159], v[164:167], v[96:111]\n"
    "ds_read_b64_tr_b16 v[136:137], v30 offset:40960\n"
    "ds_read_b64_tr_b16 v[138:139], v30 offset:41984\n"
    "s_waitcnt lgkmcnt(15)\n"
    "v_mfma_f32_32x32x16_bf16 v[112:127], v[160:163], v[164:167], v[112:127]\n"
    "ds_read_b64_tr_b16 v[140:141], v31 offset:40960\n"
    "ds_read_b64_tr_b16 v[142:143], v31 offset:41984\n"
    "s_waitcnt lgkmcnt(15)\n"
    "v_mfma_f32_32x32x16_bf16 v[64:79], v[168:171], v[184:187], v[64:79]\n"
    "ds_read_b128 v[164:167], v19 offset:32768\n"
    "ds_read_b64_tr_b16 v[148:149], v28 offset:45056\n"
    "ds_read_b64_tr_b16 v[150:151], v28 offset:46080\n"
    "s_waitcnt lgkmcnt(15)\n"
    "v_mfma_f32_32x32x16_bf16 v[80:95], v[172:175], v[184:187], v[80:95]\n"
    "ds_read_b64_tr_b16 v[152:153], v29 offset:45056\n"
    "ds_read_b64_tr_b16 v[154:155], v29 offset:46080\n"
    "s_waitcnt lgkmcnt(15)\n"
    "v_mfma_f32_32x32x16_bf16 v[96:111], v[176:179], v[184:187], v[96:111]\n"
    "ds_read_b64_tr_b16 v[156:157], v30 offset:45056\n"
    "ds_read_b64_tr_b16 v[158:159], v30 offset:46080\n"
    "s_waitcnt lgkmcnt(15)\n"
    "v_mfma_f32_32x32x16_bf16 v[112:127], v[180:183], v[184:187], v[112:127]\n"
    "ds_read_b64_tr_b16 v[160:161], v31 offset:45056\n"
    "ds_read_b64_tr_b16 v[162:163], v31 offset:46080\n"
    "s_barrier\n"
    "s_waitcnt lgkmcnt(15)\n"
    "v_mfma_f32_32x32x16_bf16 v[64:79], v[128:131], v[144:147], v[64:79]\n"
    "ds_read_b128 v[184:187], v20 offset:0\n"
    "ds_read_b64_tr_b16 v[168:169], v32 offset:0\n"
    "ds_read_b64_tr_b16 v[170:171], v32 offset:1024\n"
    "s_waitcnt lgkmcnt(15)\n"
    "v_mfma_f32_32x32x16_bf16 v[80:95], v[132:135], v[144:147], v[80:95]\n"
    "ds_read_b64_tr_b16 v[172:173], v33 offset:0\n"
    "ds_read_b64_tr_b16 v[174:175], v33 offset:1024\n"
    "s_waitcnt lgkmcnt(15)\n"
    "v_mfma_f32_32x32x16_bf16 v[96:111], v[136:139], v[144:147], v[96:111]\n"
    "ds_read_b64_tr_b16 v[176:177], v34 offset:0\n"
    "ds_read_b64_tr_b16 v[178:179], v34 offset:1024\n"
    "s_waitcnt lgkmcnt(15)\n"
    "v_mfma_f32_32x32x16_bf16 v[112:127], v[140:143], v[144:147], v[112:127]\n"
    "ds_read_b64_tr_b16 v[180:181], v35 offset:0\n"
    "ds_read_b64_tr_b16 v[182:183], v35 offset:1024\n"
    "s_waitcnt lgkmcnt(15)\n"
    "v_mfma_f32_32x32x16_bf16 v[64:79], v[148:151], v[164:167], v[64:79]\n"
    "ds_read_b128 v[144:147], v21 offset:0\n"
    "ds_read_b64_tr_b16 v[128:129], v32 offset:4096\n"
    "ds_read_b64_tr_b16 v[130:131], v32 offset:5120\n"
    "s_waitcnt lgkmcnt(15)\n"
    "v_mfma_f32_32x32x16_bf16 v[80:95], v[152:155], v[164:167], v[80:95]\n"
    "ds_read_b64_tr_b16 v[132:133], v33 offset:4096\n"
    "ds_read_b64_tr_b16 v[134:135], v33 offset:5120\n"
    "s_waitcnt lgkmcnt(15)\n"
    "v_mfma_f32_32x32x16_bf16 v[96:111], v[156:159], v[164:167], v[96:111]\n"
    "ds_read_b64_tr_b16 v[136:137], v34 offset:4096\n"
    "ds_read_b64_tr_b16 v[138:139], v34 offset:5120\n"
    "s_waitcnt lgkmcnt(15)\n"
    "v_mfma_f32_32x32x16_bf16 v[112:127], v[160:163], v[164:167], v[112:127]\n"
    "ds_read_b64_tr_b16 v[140:141], v35 offset:4096\n"
    "ds_read_b64_tr_b16 v[142:143], v35 offset:5120\n"
    "s_waitcnt lgkmcnt(15)\n"
    "v_mfma_f32_32x32x16_bf16 v[64:79], v[168:171], v[184:187], v[64:79]\n"
    "ds_read_b128 v[164:167], v22 offset:0\n"
    "ds_read_b64_tr_b16 v[148:149], v32 offset:8192\n"
    "ds_read_b64_tr_b16 v[150:151], v32 offset:9216\n"
    "s_waitcnt lgkmcnt(15)\n"
    "v_mfma_f32_32x32x16_bf16 v[80:95], v[172:175], v[184:187], v[80:95]\n"
    "ds_read_b64_tr_b16 v[152:153], v33 offset:8192\n"
    "ds_read_b64_tr_b16 v[154:155], v33 offset:9216\n"
    "s_waitcnt lgkmcnt(15)\n"
    "v_mfma_f32_32x32x16_bf16 v[96:111], v[176:179], v[184:187], v[96:111]\n"
    "ds_read_b64_tr_b16 v[156:157], v34 offset:8192\n"
    "ds_read_b64_tr_b16 v[158:159], v34 offset:9216\n"
    "s_waitcnt lgkmcnt(15)\n"
    "v_mfma_f32_32x32x16_bf16 v[112:127], v[180:183], v[184:187], v[112:127]\n"
    "ds_read_b64_tr_b16 v[160:161], v35 offset:8192\n"
    "ds_read_b64_tr_b16 v[162:163], v35 offset:9216\n"
    "s_waitcnt lgkmcnt(15)\n"
    "v_mfma_f32_32x32x16_bf16 v[64:79], v[128:131], v[144:147], v[64:79]\n"
    "ds_read_b128 v[184:187], v23 offset:0\n"
    "ds_read_b64_tr_b16 v[168:169], v32 offset:12288\n"
    "ds_read_b64_tr_b16 v[170:171], v32 offset:13312\n"
    "s_waitcnt lgkmcnt(15)\n"
    "v_mfma_f32_32x32x16_bf16 v[80:95], v[132:135], v[144:147], v[80:95]\n"
    "ds_read_b64_tr_b16 v[172:173], v33 offset:12288\n"
    "ds_read_b64_tr_b16 v[174:175], v33 offset:13312\n"
    "s_waitcnt lgkmcnt(15)\n"
    "v_mfma_f32_32x32x16_bf16 v[96:111], v[136:139], v[144:147], v[96:111]\n"
    "ds_read_b64_tr_b16 v[176:177], v34 offset:12288\n"
    "ds_read_b64_tr_b16 v[178:179], v34 offset:13312\n"
    "s_waitcnt lgkmcnt(15)\n"
    "v_mfma_f32_32x32x16_bf16 v[112:127], v[140:143], v[144:147], v[112:127]\n"
    "ds_read_b64_tr_b16 v[180:181], v35 offset:12288\n"
    "ds_read_b64_tr_b16 v[182:183], v35 offset:13312\n"
    "s_barrier\n"
    "s_waitcnt lgkmcnt(15)\n"
    "v_mfma_f32_32x32x16_bf16 v[64:79], v[148:151], v[164:167], v[64:79]\n"
    "ds_read_b128 v[144:147], v20 offset:32768\n"
    "ds_read_b64_tr_b16 v[128:129], v32 offset:32768\n"
    "ds_read_b64_tr_b16 v[130:131], v32 offset:33792\n"
    "s_waitcnt lgkmcnt(15)\n"
    "v_mfma_f32_32x32x16_bf16 v[80:95], v[152:155], v[164:167], v[80:95]\n"
    "ds_read_b64_tr_b16 v[132:133], v33 offset:32768\n"
    "ds_read_b64_tr_b16 v[134:135], v33 offset:33792\n"
    "s_waitcnt lgkmcnt(15)\n"
    "v_mfma_f32_32x32x16_bf16 v[96:111], v[156:159], v[164:167], v[96:111]\n"
    "ds_read_b64_tr_b16 v[136:137], v34 offset:32768\n"
    "ds_read_b64_tr_b16 v[138:139], v34 offset:33792\n"
    "s_waitcnt lgkmcnt(15)\n"
    "v_mfma_f32_32x32x16_bf16 v[112:127], v[160:163], v[164:167], v[112:127]\n"
    "ds_read_b64_tr_b16 v[140:141], v35 offset:32768\n"
    "ds_read_b64_tr_b16 v[142:143], v35 offset:33792\n"
    "s_waitcnt lgkmcnt(15)\n"
    "v_mfma_f32_32x32x16_bf16 v[64:79], v[168:171], v[184:187], v[64:79]\n"
    "ds_read_b128 v[164:167], v21 offset:32768\n"
    "ds_read_b64_tr_b16 v[148:149], v32 offset:36864\n"
    "ds_read_b64_tr_b16 v[150:151], v32 offset:37888\n"
    "s_waitcnt lgkmcnt(15)\n"
    "v_mfma_f32_32x32x16_bf16 v[80:95], v[172:175], v[184:187], v[80:95]\n"
    "ds_read_b64_tr_b16 v[152:153], v33 offset:36864\n"
    "ds_read_b64_tr_b16 v[154:155], v33 offset:37888\n"
    "s_waitcnt lgkmcnt(15)\n"
    "v_mfma_f32_32x32x16_bf16 v[96:111], v[176:179], v[184:187], v[96:111]\n"
    "ds_read_b64_tr_b16 v[156:157], v34 offset:36864\n"
    "ds_read_b64_tr_b16 v[158:159], v34 offset:37888\n"
    "s_waitcnt lgkmcnt(15)\n"
    "v_mfma_f32_32x32x16_bf16 v[112:127], v[180:183], v[184:187], v[112:127]\n"
    "ds_read_b64_tr_b16 v[160:161], v35 offset:36864\n"
    "ds_read_b64_tr_b16 v[162:163], v35 offset:37888\n"
    "s_waitcnt lgkmcnt(15)\n"
    "v_mfma_f32_32x32x16_bf16 v[64:79], v[128:131], v[144:147], v[64:79]\n"
    "ds_read_b128 v[184:187], v22 offset:32768\n"
    "ds_read_b64_tr_b16 v[168:169], v32 offset:40960\n"
    "ds_read_b64_tr_b16 v[170:171], v32 offset:41984\n"
    "s_waitcnt lgkmcnt(15)\n"
    "v_mfma_f32_32x32x16_bf16 v[80:95], v[132:135], v[144:147], v[80:95]\n"
    "ds_read_b64_tr_b16 v[172:173], v33 offset:40960\n"
    "ds_read_b64_tr_b16 v[174:175], v33 offset:41984\n"
    "s_waitcnt lgkmcnt(15)\n"
    "v_mfma_f32_32x32x16_bf16 v[96:111], v[136:139], v[144:147], v[96:111]\n"
    "ds_read_b64_tr_b16 v[176:177], v34 offset:40960\n"
    "ds_read_b64_tr_b16 v[178:179], v34 offset:41984\n"
    "s_waitcnt lgkmcnt(15)\n"
    "v_mfma_f32_32x32x16_bf16 v[112:127], v[140:143], v[144:147], v[112:127]\n"
    "ds_read_b64_tr_b16 v[180:181], v35 offset:40960\n"
    "ds_read_b64_tr_b16 v[182:183], v35 offset:41984\n"
    "s_waitcnt lgkmcnt(15)\n"
    "v_mfma_f32_32x32x16_bf16 v[64:79], v[148:151], v[164:167], v[64:79]\n"
    "ds_read_b128 v[144:147], v23 offset:32768\n"
    "ds_read_b64_tr_b16 v[128:129], v32 offset:45056\n"
    "ds_read_b64_tr_b16 v[130:131], v32 offset:46080\n"
    "s_waitcnt lgkmcnt(15)\n"
    "v_mfma_f32_32x32x16_bf16 v[80:95], v[152:155], v[164:167], v[80:95]\n"
    "ds_read_b64_tr_b16 v[132:133], v33 offset:45056\n"
    "ds_read_b64_tr_b16 v[134:135], v33 offset:46080\n"
    "s_waitcnt lgkmcnt(15)\n"
    "v_mfma_f32_32x32x16_bf16 v[96:111], v[156:159], v[164:167], v[96:111]\n"
    "ds_read_b64_tr_b16 v[136:137], v34 offset:45056\n"
    "ds_read_b64_tr_b16 v[138:139], v34 offset:46080\n"
    "s_waitcnt lgkmcnt(15)\n"
    "v_mfma_f32_32x32x16_bf16 v[112:127], v[160:163], v[164:167], v[112:127]\n"
    "ds_read_b64_tr_b16 v[140:141], v35 offset:45056\n"
    "ds_read_b64_tr_b16 v[142:143], v35 offset:46080\n"
    "s_barrier\n"
    "s_waitcnt lgkmcnt(15)\n"
    "v_mfma_f32_32x32x16_bf16 v[64:79], v[168:171], v[184:187], v[64:79]\n"
    "ds_read_b128 v[164:167], v24 offset:0\n"
    "ds_read_b64_tr_b16 v[148:149], v36 offset:0\n"
    "ds_read_b64_tr_b16 v[150:151], v36 offset:1024\n"
    "s_waitcnt lgkmcnt(15)\n"
    "v_mfma_f32_32x32x16_bf16 v[80:95], v[172:175], v[184:187], v[80:95]\n"
    "ds_read_b64_tr_b16 v[152:153], v37 offset:0\n"
    "ds_read_b64_tr_b16 v[154:155], v37 offset:1024\n"
    "s_waitcnt lgkmcnt(15)\n"
    "v_mfma_f32_32x32x16_bf16 v[96:111], v[176:179], v[184:187], v[96:111]\n"
    "ds_read_b64_tr_b16 v[156:157], v38 offset:0\n"
    "ds_read_b64_tr_b16 v[158:159], v38 offset:1024\n"
    "s_waitcnt lgkmcnt(15)\n"
    "v_mfma_f32_32x32x16_bf16 v[112:127], v[180:183], v[184:187], v[112:127]\n"
    "ds_read_b64_tr_b16 v[160:161], v39 offset:0\n"
    "ds_read_b64_tr_b16 v[162:163], v39 offset:1024\n"
    "s_waitcnt lgkmcnt(15)\n"
    "v_mfma_f32_32x32x16_bf16 v[64:79], v[128:131], v[144:147], v[64:79]\n"
    "ds_read_b128 v[184:187], v25 offset:0\n"
    "ds_read_b64_tr_b16 v[168:169], v36 offset:4096\n"
    "ds_read_b64_tr_b16 v[170:171], v36 offset:5120\n"
    "s_waitcnt lgkmcnt(15)\n"
    "v_mfma_f32_32x32x16_bf16 v[80:95], v[132:135], v[144:147], v[80:95]\n"
    "ds_read_b64_tr_b16 v[172:173], v37 offset:4096\n"
    "ds_read_b64_tr_b16 v[174:175], v37 offset:5120\n"
    "s_waitcnt lgkmcnt(15)\n"
    "v_mfma_f32_32x32x16_bf16 v[96:111], v[136:139], v[144:147], v[96:111]\n"
    "ds_read_b64_tr_b16 v[176:177], v38 offset:4096\n"
    "ds_read_b64_tr_b16 v[178:179], v38 offset:5120\n"
    "s_waitcnt lgkmcnt(15)\n"
    "v_mfma_f32_32x32x16_bf16 v[112:127], v[140:143], v[144:147], v[112:127]\n"
    "ds_read_b64_tr_b16 v[180:181], v39 offset:4096\n"
    "ds_read_b64_tr_b16 v[182:183], v39 offset:5120\n"
    "s_waitcnt lgkmcnt(15)\n"
    "v_mfma_f32_32x32x16_bf16 v[64:79], v[148:151], v[164:167], v[64:79]\n"
    "ds_read_b128 v[144:147], v26 offset:0\n"
    "ds_read_b64_tr_b16 v[128:129], v36 offset:8192\n"
    "ds_read_b64_tr_b16 v[130:131], v36 offset:9216\n"
    "s_waitcnt lgkmcnt(15)\n"
    "v_mfma_f32_32x32x16_bf16 v[80:95], v[152:155], v[164:167], v[80:95]\n"
    "ds_read_b64_tr_b16 v[132:133], v37 offset:8192\n"
    "ds_read_b64_tr_b16 v[134:135], v37 offset:9216\n"
    "s_waitcnt lgkmcnt(15)\n"
    "v_mfma_f32_32x32x16_bf16 v[96:111], v[156:159], v[164:167], v[96:111]\n"
    "ds_read_b64_tr_b16 v[136:137], v38 offset:8192\n"
    "ds_read_b64_tr_b16 v[138:139], v38 offset:9216\n"
    "s_waitcnt lgkmcnt(15)\n"
    "v_mfma_f32_32x32x16_bf16 v[112:127], v[160:163], v[164:167], v[112:127]\n"
    "ds_read_b64_tr_b16 v[140:141], v39 offset:8192\n"
    "ds_read_b64_tr_b16 v[142:143], v39 offset:9216\n"
    "s_waitcnt lgkmcnt(15)\n"
    "v_mfma_f32_32x32x16_bf16 v[64:79], v[168:171], v[184:187], v[64:79]\n"
    "ds_read_b128 v[164:167], v27 offset:0\n"
    "ds_read_b64_tr_b16 v[148:149], v36 offset:12288\n"
    "ds_read_b64_tr_b16 v[150:151], v36 offset:13312\n"
    "s_waitcnt lgkmcnt(15)\n"
    "v_mfma_f32_32x32x16_bf16 v[80:95], v[172:175], v[184:187], v[80:95]\n"
    "ds_read_b64_tr_b16 v[152:153], v37 offset:12288\n"
    "ds_read_b64_tr_b16 v[154:155], v37 offset:13312\n"
    "s_waitcnt lgkmcnt(15)\n"
    "v_mfma_f32_32x32x16_bf16 v[96:111], v[176:179], v[184:187], v[96:111]\n"
    "ds_read_b64_tr_b16 v[156:157], v38 offset:12288\n"
    "ds_read_b64_tr_b16 v[158:159], v38 offset:13312\n"
    "s_waitcnt lgkmcnt(15)\n"
    "v_mfma_f32_32x32x16_bf16 v[112:127], v[180:183], v[184:187], v[112:127]\n"
    "ds_read_b64_tr_b16 v[160:161], v39 offset:12288\n"
    "ds_read_b64_tr_b16 v[162:163], v39 offset:13312\n"
    "s_barrier\n"
    "s_waitcnt lgkmcnt(15)\n"
    "v_mfma_f32_32x32x16_bf16 v[64:79], v[128:131], v[144:147], v[64:79]\n"
    "ds_read_b128 v[184:187], v16 offset:0\n"
    "ds_read_b64_tr_b16 v[168:169], v28 offset:0\n"
    "ds_read_b64_tr_b16 v[170:171], v28 offset:1024\n"
    "s_waitcnt lgkmcnt(15)\n"
    "v_mfma_f32_32x32x16_bf16 v[80:95], v[132:135], v[144:147], v[80:95]\n"
    "ds_read_b64_tr_b16 v[172:173], v29 offset:0\n"
    "ds_read_b64_tr_b16 v[174:175], v29 offset:1024\n"
    "s_waitcnt lgkmcnt(15)\n"
    "v_mfma_f32_32x32x16_bf16 v[96:111], v[136:139], v[144:147], v[96:111]\n"
    "ds_read_b64_tr_b16 v[176:177], v30 offset:0\n"
    "ds_read_b64_tr_b16 v[178:179], v30 offset:1024\n"
    "s_waitcnt lgkmcnt(15)\n"
    "v_mfma_f32_32x32x16_bf16 v[112:127], v[140:143], v[144:147], v[112:127]\n"
    "ds_read_b64_tr_b16 v[180:181], v31 offset:0\n"
    "ds_read_b64_tr_b16 v[182:183], v31 offset:1024\n"
    "s_waitcnt lgkmcnt(15)\n"
    "v_mfma_f32_32x32x16_bf16 v[64:79], v[148:151], v[164:167], v[64:79]\n"
    "ds_read_b128 v[144:147], v17 offset:0\n"
    "ds_read_b64_tr_b16 v[128:129], v28 offset:4096\n"
    "ds_read_b64_tr_b16 v[130:131], v28 offset:5120\n"
    "s_waitcnt lgkmcnt(15)\n"
    "v_mfma_f32_32x32x16_bf16 v[80:95], v[152:155], v[164:167], v[80:95]\n"
    "ds_read_b64_tr_b16 v[132:133], v29 offset:4096\n"
    "ds_read_b64_tr_b16 v[134:135], v29 offset:5120\n"
    "s_waitcnt lgkmcnt(15)\n"
    "v_mfma_f32_32x32x16_bf16 v[96:111], v[156:159], v[164:167], v[96:111]\n"
    "ds_read_b64_tr_b16 v[136:137], v30 offset:4096\n"
    "ds_read_b64_tr_b16 v[138:139], v30 offset:5120\n"
    "s_waitcnt lgkmcnt(15)\n"
    "v_mfma_f32_32x32x16_bf16 v[112:127], v[160:163], v[164:167], v[112:127]\n"
    "ds_read_b64_tr_b16 v[140:141], v31 offset:4096\n"
    "ds_read_b64_tr_b16 v[142:143], v31 offset:5120\n"
    "s_waitcnt lgkmcnt(15)\n"
    "v_mfma_f32_32x32x16_bf16 v[64:79], v[168:171], v[184:187], v[64:79]\n"
    "ds_read_b128 v[164:167], v18 offset:0\n"
    "ds_read_b64_tr_b16 v[148:149], v28 offset:8192\n"
    "ds_read_b64_tr_b16 v[150:151], v28 offset:9216\n"
    "s_waitcnt lgkmcnt(15)\n"
    "v_mfma_f32_32x32x16_bf16 v[80:95], v[172:175], v[184:187], v[80:95]\n"
    "ds_read_b64_tr_b16 v[152:153], v29 offset:8192\n"
    "ds_read_b64_tr_b16 v[154:155], v29 offset:9216\n"
    "s_waitcnt lgkmcnt(15)\n"
    "v_mfma_f32_32x32x16_bf16 v[96:111], v[176:179], v[184:187], v[96:111]\n"
    "ds_read_b64_tr_b16 v[156:157], v30 offset:8192\n"
    "ds_read_b64_tr_b16 v[158:159], v30 offset:9216\n"
    "s_waitcnt lgkmcnt(15)\n"
    "v_mfma_f32_32x32x16_bf16 v[112:127], v[180:183], v[184:187], v[112:127]\n"
    "ds_read_b64_tr_b16 v[160:161], v31 offset:8192\n"
    "ds_read_b64_tr_b16 v[162:163], v31 offset:9216\n"
    "s_waitcnt lgkmcnt(15)\n"
    "v_mfma_f32_32x32x16_bf16 v[64:79], v[128:131], v[144:147], v[64:79]\n"
    "ds_read_b128 v[184:187], v19 offset:0\n"
    "ds_read_b64_tr_b16 v[168:169], v28 offset:12288\n"
    "ds_read_b64_tr_b16 v[170:171], v28 offset:13312\n"
    "s_waitcnt lgkmcnt(15)\n"
    "v_mfma_f32_32x32x16_bf16 v[80:95], v[132:135], v[144:147], v[80:95]\n"
    "ds_read_b64_tr_b16 v[172:173], v29 offset:12288\n"
    "ds_read_b64_tr_b16 v[174:175], v29 offset:13312\n"
    "s_waitcnt lgkmcnt(15)\n"
    "v_mfma_f32_32x32x16_bf16 v[96:111], v[136:139], v[144:147], v[96:111]\n"
    "ds_read_b64_tr_b16 v[176:177], v30 offset:12288\n"
    "ds_read_b64_tr_b16 v[178:179], v30 offset:13312\n"
    "s_waitcnt lgkmcnt(15)\n"
    "v_mfma_f32_32x32x16_bf16 v[112:127], v[140:143], v[144:147], v[112:127]\n"
    "ds_read_b64_tr_b16 v[180:181], v31 offset:12288\n"
    "ds_read_b64_tr_b16 v[182:183], v31 offset:13312\n"
    "s_barrier\n"
    "s_waitcnt lgkmcnt(15)\n"
    "v_mfma_f32_32x32x16_bf16 v[64:79], v[148:151], v[164:167], v[64:79]\n"
    "ds_read_b128 v[144:147], v16 offset:32768\n"
    "ds_read_b64_tr_b16 v[128:129], v28 offset:32768\n"
    "ds_read_b64_tr_b16 v[130:131], v28 offset:33792\n"
    "s_waitcnt lgkmcnt(15)\n"
    "v_mfma_f32_32x32x16_bf16 v[80:95], v[152:155], v[164:167], v[80:95]\n"
    "ds_read_b64_tr_b16 v[132:133], v29 offset:32768\n"
    "ds_read_b64_tr_b16 v[134:135], v29 offset:33792\n"
    "s_waitcnt lgkmcnt(15)\n"
    "v_mfma_f32_32x32x16_bf16 v[96:111], v[156:159], v[164:167], v[96:111]\n"
    "ds_read_b64_tr_b16 v[136:137], v30 offset:32768\n"
    "ds_read_b64_tr_b16 v[138:139], v30 offset:33792\n"
    "s_waitcnt lgkmcnt(15)\n"
    "v_mfma_f32_32x32x16_bf16 v[112:127], v[160:163], v[164:167], v[112:127]\n"
    "ds_read_b64_tr_b16 v[140:141], v31 offset:32768\n"
    "ds_read_b64_tr_b16 v[142:143], v31 offset:33792\n"
    "s_waitcnt lgkmcnt(15)\n"
    "v_mfma_f32_32x32x16_bf16 v[64:79], v[168:171], v[184:187], v[64:79]\n"
    "ds_read_b128 v[164:167], v17 offset:32768\n"
    "ds_read_b64_tr_b16 v[148:149], v28 offset:36864\n"
    "ds_read_b64_tr_b16 v[150:151], v28 offset:37888\n"
    "s_waitcnt lgkmcnt(15)\n"
    "v_mfma_f32_32x32x16_bf16 v[80:95], v[172:175], v[184:187], v[80:95]\n"
    "ds_read_b64_tr_b16 v[152:153], v29 offset:36864\n"
    "ds_read_b64_tr_b16 v[154:155], v29 offset:37888\n"
    "s_waitcnt lgkmcnt(15)\n"
    "v_mfma_f32_32x32x16_bf16 v[96:111], v[176:179], v[184:187], v[96:111]\n"
    "ds_read_b64_tr_b16 v[156:157], v30 offset:36864\n"
    "ds_read_b64_tr_b16 v[158:159], v30 offset:37888\n"
    "s_waitcnt lgkmcnt(15)\n"
    "v_mfma_f32_32x32x16_bf16 v[112:127], v[180:183], v[184:187], v[112:127]\n"
    "ds_read_b64_tr_b16 v[160:161], v31 offset:36864\n"
    "ds_read_b64_tr_b16 v[162:163], v31 offset:37888\n"
    "s_waitcnt lgkmcnt(15)\n"
    "v_mfma_f32_32x32x16_bf16 v[64:79], v[128:131], v[144:147], v[64:79]\n"
    "ds_read_b128 v[184:187], v18 offset:32768\n"
    "ds_read_b64_tr_b16 v[168:169], v28 offset:40960\n"
    "ds_read_b64_tr_b16 v[170:171], v28 offset:41984\n"
    "s_waitcnt lgkmcnt(15)\n"
    "v_mfma_f32_32x32x16_bf16 v[80:95], v[132:135], v[144:147], v[80:95]\n"
    "ds_read_b64_tr_b16 v[172:173], v29 offset:40960\n"
    "ds_read_b64_tr_b16 v[174:175], v29 offset:41984\n"
    "s_waitcnt lgkmcnt(15)\n"
    "v_mfma_f32_32x32x16_bf16 v[96:111], v[136:139], v[144:147], v[96:111]\n"
    "ds_read_b64_tr_b16 v[176:177], v30 offset:40960\n"
    "ds_read_b64_tr_b16 v[178:179], v30 offset:41984\n"
    "s_waitcnt lgkmcnt(15)\n"
    "v_mfma_f32_32x32x16_bf16 v[112:127], v[140:143], v[144:147], v[112:127]\n"
    "ds_read_b64_tr_b16 v[180:181], v31 offset:40960\n"
    "ds_read_b64_tr_b16 v[182:183], v31 offset:41984\n"
    "s_waitcnt lgkmcnt(15)\n"
    "v_mfma_f32_32x32x16_bf16 v[64:79], v[148:151], v[164:167], v[64:79]\n"
    "ds_read_b128 v[144:147], v19 offset:32768\n"
    "ds_read_b64_tr_b16 v[128:129], v28 offset:45056\n"
    "ds_read_b64_tr_b16 v[130:131], v28 offset:46080\n"
    "s_waitcnt lgkmcnt(15)\n"
    "v_mfma_f32_32x32x16_bf16 v[80:95], v[152:155], v[164:167], v[80:95]\n"
    "ds_read_b64_tr_b16 v[132:133], v29 offset:45056\n"
    "ds_read_b64_tr_b16 v[134:135], v29 offset:46080\n"
    "s_waitcnt lgkmcnt(15)\n"
    "v_mfma_f32_32x32x16_bf16 v[96:111], v[156:159], v[164:167], v[96:111]\n"
    "ds_read_b64_tr_b16 v[136:137], v30 offset:45056\n"
    "ds_read_b64_tr_b16 v[138:139], v30 offset:46080\n"
    "s_waitcnt lgkmcnt(15)\n"
    "v_mfma_f32_32x32x16_bf16 v[112:127], v[160:163], v[164:167], v[112:127]\n"
    "ds_read_b64_tr_b16 v[140:141], v31 offset:45056\n"
    "ds_read_b64_tr_b16 v[142:143], v31 offset:46080\n"
    "s_barrier\n"
    "s_waitcnt lgkmcnt(15)\n"
    "v_mfma_f32_32x32x16_bf16 v[64:79], v[168:171], v[184:187], v[64:79]\n"
    "ds_read_b128 v[164:167], v20 offset:0\n"
    "ds_read_b64_tr_b16 v[148:149], v32 offset:0\n"
    "ds_read_b64_tr_b16 v[150:151], v32 offset:1024\n"
    "s_waitcnt lgkmcnt(15)\n"
    "v_mfma_f32_32x32x16_bf16 v[80:95], v[172:175], v[184:187], v[80:95]\n"
    "ds_read_b64_tr_b16 v[152:153], v33 offset:0\n"
    "ds_read_b64_tr_b16 v[154:155], v33 offset:1024\n"
    "s_waitcnt lgkmcnt(15)\n"
    "v_mfma_f32_32x32x16_bf16 v[96:111], v[176:179], v[184:187], v[96:111]\n"
    "ds_read_b64_tr_b16 v[156:157], v34 offset:0\n"
    "ds_read_b64_tr_b16 v[158:159], v34 offset:1024\n"
    "s_waitcnt lgkmcnt(15)\n"
    "v_mfma_f32_32x32x16_bf16 v[112:127], v[180:183], v[184:187], v[112:127]\n"
    "ds_read_b64_tr_b16 v[160:161], v35 offset:0\n"
    "ds_read_b64_tr_b16 v[162:163], v35 offset:1024\n"
    "s_waitcnt lgkmcnt(15)\n"
    "v_mfma_f32_32x32x16_bf16 v[64:79], v[128:131], v[144:147], v[64:79]\n"
    "ds_read_b128 v[184:187], v21 offset:0\n"
    "ds_read_b64_tr_b16 v[168:169], v32 offset:4096\n"
    "ds_read_b64_tr_b16 v[170:171], v32 offset:5120\n"
    "s_waitcnt lgkmcnt(15)\n"
    "v_mfma_f32_32x32x16_bf16 v[80:95], v[132:135], v[144:147], v[80:95]\n"
    "ds_read_b64_tr_b16 v[172:173], v33 offset:4096\n"
    "ds_read_b64_tr_b16 v[174:175], v33 offset:5120\n"
    "s_waitcnt lgkmcnt(15)\n"
    "v_mfma_f32_32x32x16_bf16 v[96:111], v[136:139], v[144:147], v[96:111]\n"
    "ds_read_b64_tr_b16 v[176:177], v34 offset:4096\n"
    "ds_read_b64_tr_b16 v[178:179], v34 offset:5120\n"
    "s_waitcnt lgkmcnt(15)\n"
    "v_mfma_f32_32x32x16_bf16 v[112:127], v[140:143], v[144:147], v[112:127]\n"
    "ds_read_b64_tr_b16 v[180:181], v35 offset:4096\n"
    "ds_read_b64_tr_b16 v[182:183], v35 offset:5120\n"
    "s_waitcnt lgkmcnt(15)\n"
    "v_mfma_f32_32x32x16_bf16 v[64:79], v[148:151], v[164:167], v[64:79]\n"
    "ds_read_b128 v[144:147], v22 offset:0\n"
    "ds_read_b64_tr_b16 v[128:129], v32 offset:8192\n"
    "ds_read_b64_tr_b16 v[130:131], v32 offset:9216\n"
    "s_waitcnt lgkmcnt(15)\n"
    "v_mfma_f32_32x32x16_bf16 v[80:95], v[152:155], v[164:167], v[80:95]\n"
    "ds_read_b64_tr_b16 v[132:133], v33 offset:8192\n"
    "ds_read_b64_tr_b16 v[134:135], v33 offset:9216\n"
    "s_waitcnt lgkmcnt(15)\n"
    "v_mfma_f32_32x32x16_bf16 v[96:111], v[156:159], v[164:167], v[96:111]\n"
    "ds_read_b64_tr_b16 v[136:137], v34 offset:8192\n"
    "ds_read_b64_tr_b16 v[138:139], v34 offset:9216\n"
    "s_waitcnt lgkmcnt(15)\n"
    "v_mfma_f32_32x32x16_bf16 v[112:127], v[160:163], v[164:167], v[112:127]\n"
    "ds_read_b64_tr_b16 v[140:141], v35 offset:8192\n"
    "ds_read_b64_tr_b16 v[142:143], v35 offset:9216\n"
    "s_waitcnt lgkmcnt(15)\n"
    "v_mfma_f32_32x32x16_bf16 v[64:79], v[168:171], v[184:187], v[64:79]\n"
    "ds_read_b128 v[164:167], v23 offset:0\n"
    "ds_read_b64_tr_b16 v[148:149], v32 offset:12288\n"
    "ds_read_b64_tr_b16 v[150:151], v32 offset:13312\n"
    "s_waitcnt lgkmcnt(15)\n"
    "v_mfma_f32_32x32x16_bf16 v[80:95], v[172:175], v[184:187], v[80:95]\n"
    "ds_read_b64_tr_b16 v[152:153], v33 offset:12288\n"
    "ds_read_b64_tr_b16 v[154:155], v33 offset:13312\n"
    "s_waitcnt lgkmcnt(15)\n"
    "v_mfma_f32_32x32x16_bf16 v[96:111], v[176:179], v[184:187], v[96:111]\n"
    "ds_read_b64_tr_b16 v[156:157], v34 offset:12288\n"
    "ds_read_b64_tr_b16 v[158:159], v34 offset:13312\n"
    "s_waitcnt lgkmcnt(15)\n"
    "v_mfma_f32_32x32x16_bf16 v[112:127], v[180:183], v[184:187], v[112:127]\n"
    "ds_read_b64_tr_b16 v[160:161], v35 offset:12288\n"
    "ds_read_b64_tr_b16 v[162:163], v35 offset:13312\n"
    "s_waitcnt vmcnt(0)\n"
    "s_barrier\n"
    "s_waitcnt lgkmcnt(15)\n"
    "v_mfma_f32_32x32x16_bf16 v[64:79], v[128:131], v[144:147], v[64:79]\n"
    "ds_read_b128 v[224:227], v40\n"
    "s_waitcnt lgkmcnt(14)\n"
    "v_mfma_f32_32x32x16_bf16 v[80:95], v[132:135], v[144:147], v[80:95]\n"
    "ds_read_b128 v[228:231], v41\n"
    "s_waitcnt lgkmcnt(13)\n"
    "v_mfma_f32_32x32x16_bf16 v[96:111], v[136:139], v[144:147], v[96:111]\n"
    "ds_read_b128 v[232:235], v42\n"
    "s_waitcnt lgkmcnt(12)\n"
    "v_mfma_f32_32x32x16_bf16 v[112:127], v[140:143], v[144:147], v[112:127]\n"
    "ds_read_b128 v[236:239], v43\n"
    "s_waitcnt lgkmcnt(10)\n"
    "v_mfma_f32_32x32x16_bf16 v[64:79], v[148:151], v[164:167], v[64:79]\n"
    "ds_read_b128 v[240:243], v44\n"
    "s_waitcnt lgkmcnt(9)\n"
    "v_mfma_f32_32x32x16_bf16 v[80:95], v[152:155], v[164:167], v[80:95]\n"
    "ds_read_b128 v[244:247], v45\n"
    "s_waitcnt lgkmcnt(8)\n"
    "v_mfma_f32_32x32x16_bf16 v[96:111], v[156:159], v[164:167], v[96:111]\n"
    "ds_read_b128 v[248:251], v46\n"
    "s_waitcnt lgkmcnt(7)\n"
    "v_mfma_f32_32x32x16_bf16 v[112:127], v[160:163], v[164:167], v[112:127]\n"
    "ds_read_b128 v[252:255], v47\n"
    "s_nop 1\n"
    "v_cvt_pk_bf16_f32 v192, v64, v65\n"
    "v_cvt_pk_bf16_f32 v193, v66, v67\n"
    "v_cvt_pk_bf16_f32 v194, v68, v69\n"
    "v_cvt_pk_bf16_f32 v195, v70, v71\n"
    "v_cvt_pk_bf16_f32 v196, v72, v73\n"
    "v_cvt_pk_bf16_f32 v197, v74, v75\n"
    "v_cvt_pk_bf16_f32 v198, v76, v77\n"
    "v_cvt_pk_bf16_f32 v199, v78, v79\n"
    "v_permlane32_swap_b32 v192, v194\n"
    "v_permlane32_swap_b32 v193, v195\n"
    "v_permlane32_swap_b32 v196, v198\n"
    "v_permlane32_swap_b32 v197, v199\n"
    "s_waitcnt lgkmcnt(7)\n"
    "v_mfma_f32_32x32x16_bf16 v[128:143], v[224:227], v[192:195], 0\n"
    "v_cvt_pk_bf16_f32 v200, v80, v81\n"
    "v_cvt_pk_bf16_f32 v201, v82, v83\n"
    "v_cvt_pk_bf16_f32 v202, v84, v85\n"
    "v_cvt_pk_bf16_f32 v203, v86, v87\n"
    "v_cvt_pk_bf16_f32 v204, v88, v89\n"
    "v_cvt_pk_bf16_f32 v205, v90, v91\n"
    "s_waitcnt lgkmcnt(6)\n"
    "v_mfma_f32_32x32x16_bf16 v[128:143], v[228:231], v[196:199], v[128:143]\n"
    "v_cvt_pk_bf16_f32 v206, v92, v93\n"
    "v_cvt_pk_bf16_f32 v207, v94, v95\n"
    "v_permlane32_swap_b32 v200, v202\n"
    "v_permlane32_swap_b32 v201, v203\n"
    "v_permlane32_swap_b32 v204, v206\n"
    "v_permlane32_swap_b32 v205, v207\n"
    "ds_read_b128 v[224:227], v40 offset:8192\n"
    "s_waitcnt lgkmcnt(6)\n"
    "v_mfma_f32_32x32x16_bf16 v[128:143], v[232:235], v[200:203], v[128:143]\n"
    "v_cvt_pk_bf16_f32 v208, v96, v97\n"
    "v_cvt_pk_bf16_f32 v209, v98, v99\n"
    "v_cvt_pk_bf16_f32 v210, v100, v101\n"
    "v_cvt_pk_bf16_f32 v211, v102, v103\n"
    "v_cvt_pk_bf16_f32 v212, v104, v105\n"
    "v_cvt_pk_bf16_f32 v213, v106, v107\n"
    "ds_read_b128 v[228:231], v41 offset:8192\n"
    "s_waitcnt lgkmcnt(6)\n"
    "v_mfma_f32_32x32x16_bf16 v[128:143], v[236:239], v[204:207], v[128:143]\n"
    "v_cvt_pk_bf16_f32 v214, v108, v109\n"
    "v_cvt_pk_bf16_f32 v215, v110, v111\n"
    "v_permlane32_swap_b32 v208, v210\n"
    "v_permlane32_swap_b32 v209, v211\n"
    "v_permlane32_swap_b32 v212, v214\n"
    "v_permlane32_swap_b32 v213, v215\n"
    "ds_read_b128 v[232:235], v42 offset:8192\n"
    "s_waitcnt lgkmcnt(6)\n"
    "v_mfma_f32_32x32x16_bf16 v[128:143], v[240:243], v[208:211], v[128:143]\n"
    "v_cvt_pk_bf16_f32 v216, v112, v113\n"
    "v_cvt_pk_bf16_f32 v217, v114, v115\n"
    "v_cvt_pk_bf16_f32 v218, v116, v117\n"
    "v_cvt_pk_bf16_f32 v219, v118, v119\n"
    "v_cvt_pk_bf16_f32 v220, v120, v121\n"
    "v_cvt_pk_bf16_f32 v221, v122, v123\n"
    "ds_read_b128 v[236:239], v43 offset:8192\n"
    "s_waitcnt lgkmcnt(6)\n"
    "v_mfma_f32_32x32x16_bf16 v[128:143], v[244:247], v[212:215], v[128:143]\n"
    "v_cvt_pk_bf16_f32 v222, v124, v125\n"
    "v_cvt_pk_bf16_f32 v223, v126, v127\n"
    "v_permlane32_swap_b32 v216, v218\n"
    "v_permlane32_swap_b32 v217, v219\n"
    "v_permlane32_swap_b32 v220, v222\n"
    "v_permlane32_swap_b32 v221, v223\n"
    "ds_read_b128 v[240:243], v44 offset:8192\n"
    "s_waitcnt lgkmcnt(6)\n"
    "v_mfma_f32_32x32x16_bf16 v[128:143], v[248:251], v[216:219], v[128:143]\n"
    "ds_read_b128 v[244:247], v45 offset:8192\n"
    "s_waitcnt lgkmcnt(6)\n"
    "v_mfma_f32_32x32x16_bf16 v[128:143], v[252:255], v[220:223], v[128:143]\n"
    "ds_read_b128 v[248:251], v46 offset:8192\n"
    "ds_read_b128 v[252:255], v47 offset:8192\n"
    "s_waitcnt lgkmcnt(7)\n"
    "v_mfma_f32_32x32x16_bf16 v[144:159], v[224:227], v[192:195], 0\n"
    "s_waitcnt lgkmcnt(6)\n"
    "v_mfma_f32_32x32x16_bf16 v[144:159], v[228:231], v[196:199], v[144:159]\n"
    "ds_read_b128 v[224:227], v40 offset:16384\n"
    "s_waitcnt lgkmcnt(6)\n"
    "v_mfma_f32_32x32x16_bf16 v[144:159], v[232:235], v[200:203], v[144:159]\n"
    "s_nop 2\n"
    "v_max3_f32 v8, v128, v129, v130\n"
    "v_max3_f32 v9, v131, v132, v133\n"
    "ds_read_b128 v[228:231], v41 offset:16384\n"
    "s_waitcnt lgkmcnt(6)\n"
    "v_mfma_f32_32x32x16_bf16 v[144:159], v[236:239], v[204:207], v[144:159]\n"
    "v_max3_f32 v10, v134, v135, v136\n"
    "ds_read_b128 v[232:235], v42 offset:16384\n"
    "s_waitcnt lgkmcnt(6)\n"
    "v_mfma_f32_32x32x16_bf16 v[144:159], v[240:243], v[208:211], v[144:159]\n"
    "v_max3_f32 v11, v137, v138, v139\n"
    "ds_read_b128 v[236:239], v43 offset:16384\n"
    "s_waitcnt lgkmcnt(6)\n"
    "v_mfma_f32_32x32x16_bf16 v[144:159], v[244:247], v[212:215], v[144:159]\n"
    "v_max3_f32 v8, v8, v140, v141\n"
    "v_max3_f32 v9, v9, v142, v143\n"
    "ds_read_b128 v[240:243], v44 offset:16384\n"
    "s_waitcnt lgkmcnt(6)\n"
    "v_mfma_f32_32x32x16_bf16 v[144:159], v[248:251], v[216:219], v[144:159]\n"
    "v_max3_f32 v8, v8, v10, v11\n"
    "ds_read_b128 v[244:247], v45 offset:16384\n"
    "s_waitcnt lgkmcnt(6)\n"
    "v_mfma_f32_32x32x16_bf16 v[144:159], v[252:255], v[220:223], v[144:159]\n"
    "v_max_f32 v4, v8, v9\n"
    "ds_read_b128 v[248:251], v46 offset:16384\n"
    "ds_read_b128 v[252:255], v47 offset:16384\n"
    "s_waitcnt lgkmcnt(7)\n"
    "v_mfma_f32_32x32x16_bf16 v[160:175], v[224:227], v[192:195], 0\n"
    "s_waitcnt lgkmcnt(6)\n"
    "v_mfma_f32_32x32x16_bf16 v[160:175], v[228:231], v[196:199], v[160:175]\n"
    "ds_read_b128 v[224:227], v40 offset:24576\n"
    "s_waitcnt lgkmcnt(6)\n"
    "v_mfma_f32_32x32x16_bf16 v[160:175], v[232:235], v[200:203], v[160:175]\n"
    "s_nop 1\n"
    "v_max3_f32 v8, v144, v145, v146\n"
    "v_max3_f32 v9, v147, v148, v149\n"
    "ds_read_b128 v[228:231], v41 offset:24576\n"
    "s_waitcnt lgkmcnt(6)\n"
    "v_mfma_f32_32x32x16_bf16 v[160:175], v[236:239], v[204:207], v[160:175]\n"
    "v_max3_f32 v10, v150, v151, v152\n"
    "ds_read_b128 v[232:235], v42 offset:24576\n"
    "s_waitcnt lgkmcnt(6)\n"
    "v_mfma_f32_32x32x16_bf16 v[160:175], v[240:243], v[208:211], v[160:175]\n"
    "v_max3_f32 v11, v153, v154, v155\n"
    "ds_read_b128 v[236:239], v43 offset:24576\n"
    "s_waitcnt lgkmcnt(6)\n"
    "v_mfma_f32_32x32x16_bf16 v[160:175], v[244:247], v[212:215], v[160:175]\n"
    "v_max3_f32 v8, v8, v156, v157\n"
    "v_max3_f32 v9, v9, v158, v159\n"
    "ds_read_b128 v[240:243], v44 offset:24576\n"
    "s_waitcnt lgkmcnt(6)\n"
    "v_mfma_f32_32x32x16_bf16 v[160:175], v[248:251], v[216:219], v[160:175]\n"
    "v_max3_f32 v8, v8, v10, v11\n"
    "ds_read_b128 v[244:247], v45 offset:24576\n"
    "s_waitcnt lgkmcnt(6)\n"
    "v_mfma_f32_32x32x16_bf16 v[160:175], v[252:255], v[220:223], v[160:175]\n"
    "v_max3_f32 v4, v4, v8, v9\n"
    "ds_read_b128 v[248:251], v46 offset:24576\n"
    "ds_read_b128 v[252:255], v47 offset:24576\n"
    "s_waitcnt lgkmcnt(7)\n"
    "v_mfma_f32_32x32x16_bf16 v[176:191], v[224:227], v[192:195], 0\n"
    "s_waitcnt lgkmcnt(6)\n"
    "v_mfma_f32_32x32x16_bf16 v[176:191], v[228:231], v[196:199], v[176:191]\n"
    "s_waitcnt lgkmcnt(5)\n"
    "v_mfma_f32_32x32x16_bf16 v[176:191], v[232:235], v[200:203], v[176:191]\n"
    "s_nop 2\n"
    "v_max3_f32 v8, v160, v161, v162\n"
    "v_max3_f32 v9, v163, v164, v165\n"
    "s_waitcnt lgkmcnt(4)\n"
    "v_mfma_f32_32x32x16_bf16 v[176:191], v[236:239], v[204:207], v[176:191]\n"
    "v_max3_f32 v10, v166, v167, v168\n"
    "s_waitcnt lgkmcnt(3)\n"
    "v_mfma_f32_32x32x16_bf16 v[176:191], v[240:243], v[208:211], v[176:191]\n"
    "v_max3_f32 v11, v169, v170, v171\n"
    "s_waitcnt lgkmcnt(2)\n"
    "v_mfma_f32_32x32x16_bf16 v[176:191], v[244:247], v[212:215], v[176:191]\n"
    "v_max3_f32 v8, v8, v172, v173\n"
    "v_max3_f32 v9, v9, v174, v175\n"
    "s_waitcnt lgkmcnt(1)\n"
    "v_mfma_f32_32x32x16_bf16 v[176:191], v[248:251], v[216:219], v[176:191]\n"
    "v_max3_f32 v8, v8, v10, v11\n"
    "s_waitcnt lgkmcnt(0)\n"
    "v_mfma_f32_32x32x16_bf16 v[176:191], v[252:255], v[220:223], v[176:191]\n"
    "v_max3_f32 v4, v4, v8, v9\n"
    "s_nop 7\n"
    "s_nop 2\n"
    "v_max3_f32 v8, v176, v177, v178\n"
    "v_max3_f32 v9, v179, v180, v181\n"
    "v_max3_f32 v10, v182, v183, v184\n"
    "v_max3_f32 v11, v185, v186, v187\n"
    "v_max3_f32 v8, v8, v188, v189\n"
    "v_max3_f32 v9, v9, v190, v191\n"
    "v_max3_f32 v8, v8, v10, v11\n"
    "v_max3_f32 v4, v4, v8, v9\n"
    "v_mov_b32 v5, v4\n"
    "s_nop 1\n"
    "v_permlane32_swap_b32 v4, v5\n"
    "v_max_f32 v4, v4, v5\n"
    "v_mul_f32 v5, s64, v4\n"
    "v_mov_b32 v32, 0x3f803f80\n"
    "v_mov_b32 v33, 0x3f803f80\n"
    "v_mov_b32 v34, 0x3f803f80\n"
    "v_mov_b32 v35, 0x3f803f80\n"
    "v_fma_f32 v128, v128, s64, -v5\n"
    "v_fma_f32 v129, v129, s64, -v5\n"
    "v_fma_f32 v130, v130, s64, -v5\n"
    "v_fma_f32 v131, v131, s64, -v5\n"
    "v_exp_f32 v128, v128\n"
    "v_exp_f32 v129, v129\n"
    "v_fma_f32 v132, v132, s64, -v5\n"
    "v_fma_f32 v133, v133, s64, -v5\n"
    "s_barrier\n"
    "ds_read_b64_tr_b16 v[224:225], v56 offset:0\n"
    "v_exp_f32 v130, v130\n"
    "v_exp_f32 v131, v131\n"
    "v_cvt_pk_bf16_f32 v192, v128, v129\n"
    "v_fma_f32 v134, v134, s64, -v5\n"
    "v_fma_f32 v135, v135, s64, -v5\n"
    "v_exp_f32 v132, v132\n"
    "v_exp_f32 v133, v133\n"
    "v_cvt_pk_bf16_f32 v193, v130, v131\n"
    "ds_read_b64_tr_b16 v[226:227], v56 offset:2048\n"
    "v_fma_f32 v136, v136, s64, -v5\n"
    "v_fma_f32 v137, v137, s64, -v5\n"
    "v_exp_f32 v134, v134\n"
    "v_exp_f32 v135, v135\n"
    "v_cvt_pk_bf16_f32 v194, v132, v133\n"
    "v_fma_f32 v138, v138, s64, -v5\n"
    "v_fma_f32 v139, v139, s64, -v5\n"
    "v_exp_f32 v136, v136\n"
    "ds_read_b64_tr_b16 v[228:229], v56 offset:4096\n"
    "v_exp_f32 v137, v137\n"
    "v_cvt_pk_bf16_f32 v195, v134, v135\n"
    "v_fma_f32 v140, v140, s64, -v5\n"
    "v_fma_f32 v141, v141, s64, -v5\n"
    "v_exp_f32 v138, v138\n"
    "v_exp_f32 v139, v139\n"
    "v_cvt_pk_bf16_f32 v196, v136, v137\n"
    "v_fma_f32 v142, v142, s64, -v5\n"
    "ds_read_b64_tr_b16 v[230:231], v56 offset:6144\n"
    "v_fma_f32 v143, v143, s64, -v5\n"
    "v_exp_f32 v140, v140\n"
    "v_exp_f32 v141, v141\n"
    "v_cvt_pk_bf16_f32 v197, v138, v139\n"
    "v_exp_f32 v142, v142\n"
    "v_exp_f32 v143, v143\n"
    "v_cvt_pk_bf16_f32 v198, v140, v141\n"
    "v_cvt_pk_bf16_f32 v199, v142, v143\n"
    "s_waitcnt lgkmcnt(2)\n"
    "v_mfma_f32_32x32x16_bf16 v[64:79], v[224:227], v[192:195], 0\n"
    "v_fma_f32 v144, v144, s64, -v5\n"
    "v_fma_f32 v145, v145, s64, -v5\n"
    "v_fma_f32 v146, v146, s64, -v5\n"
    "v_fma_f32 v147, v147, s64, -v5\n"
    "v_exp_f32 v144, v144\n"
    "v_exp_f32 v145, v145\n"
    "v_fma_f32 v148, v148, s64, -v5\n"
    "v_mfma_f32_32x32x16_bf16 v[16:31], v[32:35], v[192:195], 0\n"
    "v_fma_f32 v149, v149, s64, -v5\n"
    "v_exp_f32 v146, v146\n"
    "v_exp_f32 v147, v147\n"
    "v_cvt_pk_bf16_f32 v200, v144, v145\n"
    "v_fma_f32 v150, v150, s64, -v5\n"
    "v_fma_f32 v151, v151, s64, -v5\n"
    "v_exp_f32 v148, v148\n"
    "ds_read_b64_tr_b16 v[224:225], v56 offset:8192\n"
    "ds_read_b64_tr_b16 v[226:227], v56 offset:10240\n"
    "v_exp_f32 v149, v149\n"
    "v_cvt_pk_bf16_f32 v201, v146, v147\n"
    "v_fma_f32 v152, v152, s64, -v5\n"
    "v_fma_f32 v153, v153, s64, -v5\n"
    "v_exp_f32 v150, v150\n"
    "v_exp_f32 v151, v151\n"
    "s_waitcnt lgkmcnt(2)\n"
    "v_mfma_f32_32x32x16_bf16 v[64:79], v[228:231], v[196:199], v[64:79]\n"
    "v_cvt_pk_bf16_f32 v202, v148, v149\n"
    "v_fma_f32 v154, v154, s64, -v5\n"
    "v_fma_f32 v155, v155, s64, -v5\n"
    "v_exp_f32 v152, v152\n"
    "v_exp_f32 v153, v153\n"
    "v_cvt_pk_bf16_f32 v203, v150, v151\n"
    "v_fma_f32 v156, v156, s64, -v5\n"
    "v_mfma_f32_32x32x16_bf16 v[16:31], v[32:35], v[196:199], v[16:31]\n"
    "v_fma_f32 v157, v157, s64, -v5\n"
    "v_exp_f32 v154, v154\n"
    "v_exp_f32 v155, v155\n"
    "v_cvt_pk_bf16_f32 v204, v152, v153\n"
    "v_fma_f32 v158, v158, s64, -v5\n"
    "v_fma_f32 v159, v159, s64, -v5\n"
    "v_exp_f32 v156, v156\n"
    "ds_read_b64_tr_b16 v[228:229], v56 offset:12288\n"
    "ds_read_b64_tr_b16 v[230:231], v56 offset:14336\n"
    "v_exp_f32 v157, v157\n"
    "v_cvt_pk_bf16_f32 v205, v154, v155\n"
    "v_exp_f32 v158, v158\n"
    "v_exp_f32 v159, v159\n"
    "v_cvt_pk_bf16_f32 v206, v156, v157\n"
    "v_cvt_pk_bf16_f32 v207, v158, v159\n"
    "s_waitcnt lgkmcnt(2)\n"
    "v_mfma_f32_32x32x16_bf16 v[64:79], v[224:227], v[200:203], v[64:79]\n"
    "v_fma_f32 v160, v160, s64, -v5\n"
    "v_fma_f32 v161, v161, s64, -v5\n"
    "v_fma_f32 v162, v162, s64, -v5\n"
    "v_fma_f32 v163, v163, s64, -v5\n"
    "v_exp_f32 v160, v160\n"
    "v_exp_f32 v161, v161\n"
    "v_fma_f32 v164, v164, s64, -v5\n"
    "v_mfma_f32_32x32x16_bf16 v[16:31], v[32:35], v[200:203], v[16:31]\n"
    "v_fma_f32 v165, v165, s64, -v5\n"
    "v_exp_f32 v162, v162\n"
    "v_exp_f32 v163, v163\n"
    "v_cvt_pk_bf16_f32 v208, v160, v161\n"
    "v_fma_f32 v166, v166, s64, -v5\n"
    "v_fma_f32 v167, v167, s64, -v5\n"
    "v_exp_f32 v164, v164\n"
    "ds_read_b64_tr_b16 v[224:225], v56 offset:16384\n"
    "ds_read_b64_tr_b16 v[226:227], v56 offset:18432\n"
    "v_exp_f32 v165, v165\n"
    "v_cvt_pk_bf16_f32 v209, v162, v163\n"
    "v_fma_f32 v168, v168, s64, -v5\n"
    "v_fma_f32 v169, v169, s64, -v5\n"
    "v_exp_f32 v166, v166\n"
    "v_exp_f32 v167, v167\n"
    "s_waitcnt lgkmcnt(2)\n"
    "v_mfma_f32_32x32x16_bf16 v[64:79], v[228:231], v[204:207], v[64:79]\n"
    "v_cvt_pk_bf16_f32 v210, v164, v165\n"
    "v_fma_f32 v170, v170, s64, -v5\n"
    "v_fma_f32 v171, v171, s64, -v5\n"
    "v_exp_f32 v168, v168\n"
    "v_exp_f32 v169, v169\n"
    "v_cvt_pk_bf16_f32 v211, v166, v167\n"
    "v_fma_f32 v172, v172, s64, -v5\n"
    "v_mfma_f32_32x32x16_bf16 v[16:31], v[32:35], v[204:207], v[16:31]\n"
    "v_fma_f32 v173, v173, s64, -v5\n"
    "v_exp_f32 v170, v170\n"
    "v_exp_f32 v171, v171\n"
    "v_cvt_pk_bf16_f32 v212, v168, v169\n"
    "v_fma_f32 v174, v174, s64, -v5\n"
    "v_fma_f32 v175, v175, s64, -v5\n"
    "v_exp_f32 v172, v172\n"
    "ds_read_b64_tr_b16 v[228:229], v56 offset:20480\n"
    "ds_read_b64_tr_b16 v[230:231], v56 offset:22528\n"
    "v_exp_f32 v173, v173\n"
    "v_cvt_pk_bf16_f32 v213, v170, v171\n"
    "v_exp_f32 v174, v174\n"
    "v_exp_f32 v175, v175\n"
    "v_cvt_pk_bf16_f32 v214, v172, v173\n"
    "v_cvt_pk_bf16_f32 v215, v174, v175\n"
    "s_waitcnt lgkmcnt(2)\n"
    "v_mfma_f32_32x32x16_bf16 v[64:79], v[224:227], v[208:211], v[64:79]\n"
    "v_fma_f32 v176, v176, s64, -v5\n"
    "v_fma_f32 v177, v177, s64, -v5\n"
    "v_fma_f32 v178, v178, s64, -v5\n"
    "ds_read_b64_tr_b16 v[232:233], v57 offset:0\n"
    "v_fma_f32 v179, v179, s64, -v5\n"
    "v_exp_f32 v176, v176\n"
    "v_mfma_f32_32x32x16_bf16 v[16:31], v[32:35], v[208:211], v[16:31]\n"
    "v_exp_f32 v177, v177\n"
    "v_fma_f32 v180, v180, s64, -v5\n"
    "ds_read_b64_tr_b16 v[234:235], v57 offset:2048\n"
    "v_fma_f32 v181, v181, s64, -v5\n"
    "v_exp_f32 v178, v178\n"
    "ds_read_b64_tr_b16 v[224:225], v56 offset:24576\n"
    "ds_read_b64_tr_b16 v[226:227], v56 offset:26624\n"
    "v_exp_f32 v179, v179\n"
    "v_cvt_pk_bf16_f32 v216, v176, v177\n"
    "v_fma_f32 v182, v182, s64, -v5\n"
    "ds_read_b64_tr_b16 v[236:237], v57 offset:4096\n"
    "v_fma_f32 v183, v183, s64, -v5\n"
    "v_exp_f32 v180, v180\n"
    "s_waitcnt lgkmcnt(5)\n"
    "v_mfma_f32_32x32x16_bf16 v[64:79], v[228:231], v[212:215], v[64:79]\n"
    "v_exp_f32 v181, v181\n"
    "v_cvt_pk_bf16_f32 v217, v178, v179\n"
    "ds_read_b64_tr_b16 v[238:239], v57 offset:6144\n"
    "v_fma_f32 v184, v184, s64, -v5\n"
    "v_fma_f32 v185, v185, s64, -v5\n"
    "v_mfma_f32_32x32x16_bf16 v[16:31], v[32:35], v[212:215], v[16:31]\n"
    "v_exp_f32 v182, v182\n"
    "v_exp_f32 v183, v183\n"
    "ds_read_b64_tr_b16 v[240:241], v57 offset:8192\n"
    "v_cvt_pk_bf16_f32 v218, v180, v181\n"
    "v_fma_f32 v186, v186, s64, -v5\n"
    "v_fma_f32 v187, v187, s64, -v5\n"
    "ds_read_b64_tr_b16 v[228:229], v56 offset:28672\n"
    "ds_read_b64_tr_b16 v[230:231], v56 offset:30720\n"
    "v_exp_f32 v184, v184\n"
    "v_exp_f32 v185, v185\n"
    "ds_read_b64_tr_b16 v[242:243], v57 offset:10240\n"
    "v_cvt_pk_bf16_f32 v219, v182, v183\n"
    "v_fma_f32 v188, v188, s64, -v5\n"
    "ds_read_b64_tr_b16 v[244:245], v57 offset:12288\n"
    "v_fma_f32 v189, v189, s64, -v5\n"
    "v_exp_f32 v186, v186\n"
    "ds_read_b64_tr_b16 v[246:247], v57 offset:14336\n"
    "v_exp_f32 v187, v187\n"
    "v_cvt_pk_bf16_f32 v220, v184, v185\n"
    "v_fma_f32 v190, v190, s64, -v5\n"
    "ds_read_b64_tr_b16 v[248:249], v57 offset:16384\n"
    "v_fma_f32 v191, v191, s64, -v5\n"
    "v_exp_f32 v188, v188\n"
    "ds_read_b64_tr_b16 v[250:251], v57 offset:18432\n"
    "v_exp_f32 v189, v189\n"
    "v_cvt_pk_bf16_f32 v221, v186, v187\n"
    "ds_read_b64_tr_b16 v[252:253], v57 offset:20480\n"
    "v_exp_f32 v190, v190\n"
    "v_exp_f32 v191, v191\n"
    "ds_read_b64_tr_b16 v[254:255], v57 offset:22528\n"
    "v_cvt_pk_bf16_f32 v222, v188, v189\n"
    "v_cvt_pk_bf16_f32 v223, v190, v191\n"
    "v_mfma_f32_32x32x16_bf16 v[16:31], v[32:35], v[216:219], v[16:31]\n"
    "s_waitcnt lgkmcnt(12)\n"
    "v_mfma_f32_32x32x16_bf16 v[64:79], v[224:227], v[216:219], v[64:79]\n"
    "v_mfma_f32_32x32x16_bf16 v[16:31], v[32:35], v[220:223], v[16:31]\n"
    "s_waitcnt lgkmcnt(7)\n"
    "v_mfma_f32_32x32x16_bf16 v[64:79], v[228:231], v[220:223], v[64:79]\n"
    "v_mfma_f32_32x32x16_bf16 v[80:95], v[232:235], v[192:195], 0\n"
    "s_nop 7\n"
    "s_nop 0\n"
    "v_rcp_f32 v8, v16\n"
    "ds_read_b64_tr_b16 v[224:225], v57 offset:24576\n"
    "ds_read_b64_tr_b16 v[226:227], v57 offset:26624\n"
    "v_mfma_f32_32x32x16_bf16 v[80:95], v[236:239], v[196:199], v[80:95]\n"
    "v_mul_f32 v64, v8, v64\n"
    "v_mul_f32 v65, v8, v65\n"
    "ds_read_b64_tr_b16 v[228:229], v57 offset:28672\n"
    "v_mul_f32 v66, v8, v66\n"
    "v_mul_f32 v67, v8, v67\n"
    "ds_read_b64_tr_b16 v[230:231], v57 offset:30720\n"
    "v_mul_f32 v68, v8, v68\n"
    "s_waitcnt lgkmcnt(10)\n"
    "v_mfma_f32_32x32x16_bf16 v[80:95], v[240:243], v[200:203], v[80:95]\n"
    "v_mul_f32 v69, v8, v69\n"
    "v_mul_f32 v70, v8, v70\n"
    "ds_read_b64_tr_b16 v[232:233], v58 offset:0\n"
    "v_mul_f32 v71, v8, v71\n"
    "v_cvt_pk_bf16_f32 v128, v64, v65\n"
    "ds_read_b64_tr_b16 v[234:235], v58 offset:2048\n"
    "v_cvt_pk_bf16_f32 v129, v66, v67\n"
    "s_waitcnt lgkmcnt(10)\n"
    "v_mfma_f32_32x32x16_bf16 v[80:95], v[244:247], v[204:207], v[80:95]\n"
    "v_cvt_pk_bf16_f32 v130, v68, v69\n"
    "v_cvt_pk_bf16_f32 v131, v70, v71\n"
    "ds_read_b64_tr_b16 v[236:237], v58 offset:4096\n"
    "buffer_store_dwordx4 v[128:131], v60, s[44:47], s76 offen sc1\n"
    "v_mul_f32 v72, v8, v72\n"
    "ds_read_b64_tr_b16 v[238:239], v58 offset:6144\n"
    "v_mul_f32 v73, v8, v73\n"
    "s_waitcnt lgkmcnt(10)\n"
    "v_mfma_f32_32x32x16_bf16 v[80:95], v[248:251], v[208:211], v[80:95]\n"
    "v_mul_f32 v74, v8, v74\n"
    "v_mul_f32 v75, v8, v75\n"
    "ds_read_b64_tr_b16 v[240:241], v58 offset:8192\n"
    "v_mul_f32 v76, v8, v76\n"
    "v_mul_f32 v77, v8, v77\n"
    "ds_read_b64_tr_b16 v[242:243], v58 offset:10240\n"
    "v_mul_f32 v78, v8, v78\n"
    "s_waitcnt lgkmcnt(10)\n"
    "v_mfma_f32_32x32x16_bf16 v[80:95], v[252:255], v[212:215], v[80:95]\n"
    "v_mul_f32 v79, v8, v79\n"
    "v_cvt_pk_bf16_f32 v132, v72, v73\n"
    "ds_read_b64_tr_b16 v[244:245], v58 offset:12288\n"
    "v_cvt_pk_bf16_f32 v133, v74, v75\n"
    "v_cvt_pk_bf16_f32 v134, v76, v77\n"
    "ds_read_b64_tr_b16 v[246:247], v58 offset:14336\n"
    "v_cvt_pk_bf16_f32 v135, v78, v79\n"
    "s_waitcnt lgkmcnt(10)\n"
    "v_mfma_f32_32x32x16_bf16 v[80:95], v[224:227], v[216:219], v[80:95]\n"
    "buffer_store_dwordx4 v[132:135], v60, s[44:47], s76 offen offset:1024 sc1\n"
    "ds_read_b64_tr_b16 v[248:249], v58 offset:16384\n"
    "ds_read_b64_tr_b16 v[250:251], v58 offset:18432\n"
    "s_waitcnt lgkmcnt(10)\n"
    "v_mfma_f32_32x32x16_bf16 v[80:95], v[228:231], v[220:223], v[80:95]\n"
    "ds_read_b64_tr_b16 v[252:253], v58 offset:20480\n"
    "ds_read_b64_tr_b16 v[254:255], v58 offset:22528\n"
    "s_waitcnt lgkmcnt(10)\n"
    "v_mfma_f32_32x32x16_bf16 v[96:111], v[232:235], v[192:195], 0\n"
    "ds_read_b64_tr_b16 v[224:225], v58 offset:24576\n"
    "ds_read_b64_tr_b16 v[226:227], v58 offset:26624\n"
    "s_waitcnt lgkmcnt(10)\n"
    "v_mfma_f32_32x32x16_bf16 v[96:111], v[236:239], v[196:199], v[96:111]\n"
    "s_nop 3\n"
    "v_mul_f32 v80, v8, v80\n"
    "v_mul_f32 v81, v8, v81\n"
    "ds_read_b64_tr_b16 v[228:229], v58 offset:28672\n"
    "v_mul_f32 v82, v8, v82\n"
    "v_mul_f32 v83, v8, v83\n"
    "ds_read_b64_tr_b16 v[230:231], v58 offset:30720\n"
    "v_mul_f32 v84, v8, v84\n"
    "s_waitcnt lgkmcnt(10)\n"
    "v_mfma_f32_32x32x16_bf16 v[96:111], v[240:243], v[200:203], v[96:111]\n"
    "v_mul_f32 v85, v8, v85\n"
    "v_mul_f32 v86, v8, v86\n"
    "ds_read_b64_tr_b16 v[232:233], v59 offset:0\n"
    "v_mul_f32 v87, v8, v87\n"
    "v_cvt_pk_bf16_f32 v136, v80, v81\n"
    "ds_read_b64_tr_b16 v[234:235], v59 offset:2048\n"
    "v_cvt_pk_bf16_f32 v137, v82, v83\n"
    "s_waitcnt lgkmcnt(10)\n"
    "v_mfma_f32_32x32x16_bf16 v[96:111], v[244:247], v[204:207], v[96:111]\n"
    "v_cvt_pk_bf16_f32 v138, v84, v85\n"
    "v_cvt_pk_bf16_f32 v139, v86, v87\n"
    "ds_read_b64_tr_b16 v[236:237], v59 offset:4096\n"
    "buffer_store_dwordx4 v[136:139], v60, s[44:47], s76 offen offset:2048 sc1\n"
    "v_mul_f32 v88, v8, v88\n"
    "ds_read_b64_tr_b16 v[238:239], v59 offset:6144\n"
    "v_mul_f32 v89, v8, v89\n"
    "s_waitcnt lgkmcnt(10)\n"
    "v_mfma_f32_32x32x16_bf16 v[96:111], v[248:251], v[208:211], v[96:111]\n"
    "v_mul_f32 v90, v8, v90\n"
    "v_mul_f32 v91, v8, v91\n"
    "ds_read_b64_tr_b16 v[240:241], v59 offset:8192\n"
    "v_mul_f32 v92, v8, v92\n"
    "v_mul_f32 v93, v8, v93\n"
    "ds_read_b64_tr_b16 v[242:243], v59 offset:10240\n"
    "v_mul_f32 v94, v8, v94\n"
    "s_waitcnt lgkmcnt(10)\n"
    "v_mfma_f32_32x32x16_bf16 v[96:111], v[252:255], v[212:215], v[96:111]\n"
    "v_mul_f32 v95, v8, v95\n"
    "v_cvt_pk_bf16_f32 v140, v88, v89\n"
    "ds_read_b64_tr_b16 v[244:245], v59 offset:12288\n"
    "v_cvt_pk_bf16_f32 v141, v90, v91\n"
    "v_cvt_pk_bf16_f32 v142, v92, v93\n"
    "ds_read_b64_tr_b16 v[246:247], v59 offset:14336\n"
    "v_cvt_pk_bf16_f32 v143, v94, v95\n"
    "s_waitcnt lgkmcnt(10)\n"
    "v_mfma_f32_32x32x16_bf16 v[96:111], v[224:227], v[216:219], v[96:111]\n"
    "buffer_store_dwordx4 v[140:143], v60, s[44:47], s76 offen offset:3072 sc1\n"
    "ds_read_b64_tr_b16 v[248:249], v59 offset:16384\n"
    "ds_read_b64_tr_b16 v[250:251], v59 offset:18432\n"
    "s_waitcnt lgkmcnt(10)\n"
    "v_mfma_f32_32x32x16_bf16 v[96:111], v[228:231], v[220:223], v[96:111]\n"
    "ds_read_b64_tr_b16 v[252:253], v59 offset:20480\n"
    "ds_read_b64_tr_b16 v[254:255], v59 offset:22528\n"
    "s_waitcnt lgkmcnt(10)\n"
    "v_mfma_f32_32x32x16_bf16 v[112:127], v[232:235], v[192:195], 0\n"
    "ds_read_b64_tr_b16 v[224:225], v59 offset:24576\n"
    "ds_read_b64_tr_b16 v[226:227], v59 offset:26624\n"
    "s_waitcnt lgkmcnt(10)\n"
    "v_mfma_f32_32x32x16_bf16 v[112:127], v[236:239], v[196:199], v[112:127]\n"
    "s_nop 3\n"
    "v_mul_f32 v96, v8, v96\n"
    "v_mul_f32 v97, v8, v97\n"
    "ds_read_b64_tr_b16 v[228:229], v59 offset:28672\n"
    "v_mul_f32 v98, v8, v98\n"
    "v_mul_f32 v99, v8, v99\n"
    "ds_read_b64_tr_b16 v[230:231], v59 offset:30720\n"
    "v_mul_f32 v100, v8, v100\n"
    "s_waitcnt lgkmcnt(10)\n"
    "v_mfma_f32_32x32x16_bf16 v[112:127], v[240:243], v[200:203], v[112:127]\n"
    "v_mul_f32 v101, v8, v101\n"
    "v_mul_f32 v102, v8, v102\n"
    "v_mul_f32 v103, v8, v103\n"
    "v_cvt_pk_bf16_f32 v144, v96, v97\n"
    "v_cvt_pk_bf16_f32 v145, v98, v99\n"
    "s_waitcnt lgkmcnt(8)\n"
    "v_mfma_f32_32x32x16_bf16 v[112:127], v[244:247], v[204:207], v[112:127]\n"
    "v_cvt_pk_bf16_f32 v146, v100, v101\n"
    "v_cvt_pk_bf16_f32 v147, v102, v103\n"
    "buffer_store_dwordx4 v[144:147], v60, s[44:47], s77 offen sc1\n"
    "v_mul_f32 v104, v8, v104\n"
    "v_mul_f32 v105, v8, v105\n"
    "s_waitcnt lgkmcnt(6)\n"
    "v_mfma_f32_32x32x16_bf16 v[112:127], v[248:251], v[208:211], v[112:127]\n"
    "v_mul_f32 v106, v8, v106\n"
    "v_mul_f32 v107, v8, v107\n"
    "v_mul_f32 v108, v8, v108\n"
    "v_mul_f32 v109, v8, v109\n"
    "v_mul_f32 v110, v8, v110\n"
    "s_waitcnt lgkmcnt(4)\n"
    "v_mfma_f32_32x32x16_bf16 v[112:127], v[252:255], v[212:215], v[112:127]\n"
    "v_mul_f32 v111, v8, v111\n"
    "v_cvt_pk_bf16_f32 v148, v104, v105\n"
    "v_cvt_pk_bf16_f32 v149, v106, v107\n"
    "v_cvt_pk_bf16_f32 v150, v108, v109\n"
    "v_cvt_pk_bf16_f32 v151, v110, v111\n"
    "s_waitcnt lgkmcnt(2)\n"
    "v_mfma_f32_32x32x16_bf16 v[112:127], v[224:227], v[216:219], v[112:127]\n"
    "buffer_store_dwordx4 v[148:151], v60, s[44:47], s77 offen offset:1024 sc1\n"
    "s_waitcnt lgkmcnt(0)\n"
    "v_mfma_f32_32x32x16_bf16 v[112:127], v[228:231], v[220:223], v[112:127]\n"
    "s_nop 7\n"
    "s_nop 3\n"
    "v_mul_f32 v112, v8, v112\n"
    "v_mul_f32 v113, v8, v113\n"
    "v_mul_f32 v114, v8, v114\n"
    "v_mul_f32 v115, v8, v115\n"
    "v_mul_f32 v116, v8, v116\n"
    "v_mul_f32 v117, v8, v117\n"
    "v_mul_f32 v118, v8, v118\n"
    "v_mul_f32 v119, v8, v119\n"
    "v_cvt_pk_bf16_f32 v152, v112, v113\n"
    "v_cvt_pk_bf16_f32 v153, v114, v115\n"
    "v_cvt_pk_bf16_f32 v154, v116, v117\n"
    "v_cvt_pk_bf16_f32 v155, v118, v119\n"
    "buffer_store_dwordx4 v[152:155], v60, s[44:47], s77 offen offset:2048 sc1\n"
    "v_mul_f32 v120, v8, v120\n"
    "v_mul_f32 v121, v8, v121\n"
    "v_mul_f32 v122, v8, v122\n"
    "v_mul_f32 v123, v8, v123\n"
    "v_mul_f32 v124, v8, v124\n"
    "v_mul_f32 v125, v8, v125\n"
    "v_mul_f32 v126, v8, v126\n"
    "v_mul_f32 v127, v8, v127\n"
    "v_cvt_pk_bf16_f32 v156, v120, v121\n"
    "v_cvt_pk_bf16_f32 v157, v122, v123\n"
    "v_cvt_pk_bf16_f32 v158, v124, v125\n"
    "v_cvt_pk_bf16_f32 v159, v126, v127\n"
    "buffer_store_dwordx4 v[156:159], v60, s[44:47], s77 offen offset:3072 sc1\n"
    "s_barrier\n"
    "ds_read_b64_tr_b16 v[224:225], v48 offset:0\n"
    "ds_read_b64_tr_b16 v[226:227], v48 offset:2048\n"
    "ds_read_b64_tr_b16 v[228:229], v49 offset:0\n"
    "ds_read_b64_tr_b16 v[230:231], v49 offset:2048\n"
    "ds_read_b64_tr_b16 v[232:233], v50 offset:0\n"
    "ds_read_b64_tr_b16 v[234:235], v50 offset:2048\n"
    "ds_read_b64_tr_b16 v[236:237], v51 offset:0\n"
    "ds_read_b64_tr_b16 v[238:239], v51 offset:2048\n"
    "ds_read_b64_tr_b16 v[240:241], v48 offset:4096\n"
    "ds_read_b64_tr_b16 v[242:243], v48 offset:6144\n"
    "ds_read_b64_tr_b16 v[244:245], v49 offset:4096\n"
    "ds_read_b64_tr_b16 v[246:247], v49 offset:6144\n"
    "s_waitcnt lgkmcnt(10)\n"
    "v_mfma_f32_32x32x16_bf16 v[160:175], v[224:227], v[128:131], 0\n"
    "ds_read_b64_tr_b16 v[248:249], v50 offset:4096\n"
    "ds_read_b64_tr_b16 v[250:251], v50 offset:6144\n"
    "s_waitcnt vmcnt(0)\n"
    "buffer_store_dwordx2 v[12:13], v61, s[44:47], 0 offen sc1\n"
    "s_waitcnt lgkmcnt(10)\n"
    "v_mfma_f32_32x32x16_bf16 v[176:191], v[228:231], v[128:131], 0\n"
    "ds_read_b64_tr_b16 v[252:253], v51 offset:4096\n"
    "ds_read_b64_tr_b16 v[254:255], v51 offset:6144\n"
    "s_waitcnt lgkmcnt(10)\n"
    "v_mfma_f32_32x32x16_bf16 v[192:207], v[232:235], v[128:131], 0\n"
    "ds_read_b64_tr_b16 v[224:225], v48 offset:8192\n"
    "ds_read_b64_tr_b16 v[226:227], v48 offset:10240\n"
    "s_waitcnt lgkmcnt(10)\n"
    "v_mfma_f32_32x32x16_bf16 v[208:223], v[236:239], v[128:131], 0\n"
    "ds_read_b64_tr_b16 v[228:229], v49 offset:8192\n"
    "ds_read_b64_tr_b16 v[230:231], v49 offset:10240\n"
    "s_waitcnt lgkmcnt(10)\n"
    "v_mfma_f32_32x32x16_bf16 v[160:175], v[240:243], v[132:135], v[160:175]\n"
    "ds_read_b64_tr_b16 v[232:233], v50 offset:8192\n"
    "ds_read_b64_tr_b16 v[234:235], v50 offset:10240\n"
    "s_waitcnt lgkmcnt(10)\n"
    "v_mfma_f32_32x32x16_bf16 v[176:191], v[244:247], v[132:135], v[176:191]\n"
    "ds_read_b64_tr_b16 v[236:237], v51 offset:8192\n"
    "ds_read_b64_tr_b16 v[238:239], v51 offset:10240\n"
    "s_waitcnt lgkmcnt(10)\n"
    "v_mfma_f32_32x32x16_bf16 v[192:207], v[248:251], v[132:135], v[192:207]\n"
    "ds_read_b64_tr_b16 v[240:241], v48 offset:12288\n"
    "ds_read_b64_tr_b16 v[242:243], v48 offset:14336\n"
    "s_waitcnt lgkmcnt(10)\n"
    "v_mfma_f32_32x32x16_bf16 v[208:223], v[252:255], v[132:135], v[208:223]\n"
    "ds_read_b64_tr_b16 v[244:245], v49 offset:12288\n"
    "ds_read_b64_tr_b16 v[246:247], v49 offset:14336\n"
    "s_waitcnt lgkmcnt(10)\n"
    "v_mfma_f32_32x32x16_bf16 v[160:175], v[224:227], v[136:139], v[160:175]\n"
    "ds_read_b64_tr_b16 v[248:249], v50 offset:12288\n"
    "ds_read_b64_tr_b16 v[250:251], v50 offset:14336\n"
    "s_waitcnt lgkmcnt(10)\n"
    "v_mfma_f32_32x32x16_bf16 v[176:191], v[228:231], v[136:139], v[176:191]\n"
    "ds_read_b64_tr_b16 v[252:253], v51 offset:12288\n"
    "ds_read_b64_tr_b16 v[254:255], v51 offset:14336\n"
    "s_waitcnt lgkmcnt(10)\n"
    "v_mfma_f32_32x32x16_bf16 v[192:207], v[232:235], v[136:139], v[192:207]\n"
    "ds_read_b64_tr_b16 v[224:225], v48 offset:16384\n"
    "ds_read_b64_tr_b16 v[226:227], v48 offset:18432\n"
    "s_waitcnt lgkmcnt(10)\n"
    "v_mfma_f32_32x32x16_bf16 v[208:223], v[236:239], v[136:139], v[208:223]\n"
    "ds_read_b64_tr_b16 v[228:229], v49 offset:16384\n"
    "ds_read_b64_tr_b16 v[230:231], v49 offset:18432\n"
    "s_waitcnt lgkmcnt(10)\n"
    "v_mfma_f32_32x32x16_bf16 v[160:175], v[240:243], v[140:143], v[160:175]\n"
    "ds_read_b64_tr_b16 v[232:233], v50 offset:16384\n"
    "ds_read_b64_tr_b16 v[234:235], v50 offset:18432\n"
    "s_waitcnt lgkmcnt(10)\n"
    "v_mfma_f32_32x32x16_bf16 v[176:191], v[244:247], v[140:143], v[176:191]\n"
    "ds_read_b64_tr_b16 v[236:237], v51 offset:16384\n"
    "ds_read_b64_tr_b16 v[238:239], v51 offset:18432\n"
    "s_waitcnt lgkmcnt(10)\n"
    "v_mfma_f32_32x32x16_bf16 v[192:207], v[248:251], v[140:143], v[192:207]\n"
    "ds_read_b64_tr_b16 v[240:241], v48 offset:20480\n"
    "ds_read_b64_tr_b16 v[242:243], v48 offset:22528\n"
    "s_waitcnt lgkmcnt(10)\n"
    "v_mfma_f32_32x32x16_bf16 v[208:223], v[252:255], v[140:143], v[208:223]\n"
    "ds_read_b64_tr_b16 v[244:245], v49 offset:20480\n"
    "ds_read_b64_tr_b16 v[246:247], v49 offset:22528\n"
    "s_waitcnt lgkmcnt(10)\n"
    "v_mfma_f32_32x32x16_bf16 v[160:175], v[224:227], v[144:147], v[160:175]\n"
    "ds_read_b64_tr_b16 v[248:249], v50 offset:20480\n"
    "ds_read_b64_tr_b16 v[250:251], v50 offset:22528\n"
    "s_waitcnt lgkmcnt(10)\n"
    "v_mfma_f32_32x32x16_bf16 v[176:191], v[228:231], v[144:147], v[176:191]\n"
    "ds_read_b64_tr_b16 v[252:253], v51 offset:20480\n"
    "ds_read_b64_tr_b16 v[254:255], v51 offset:22528\n"
    "s_waitcnt lgkmcnt(10)\n"
    "v_mfma_f32_32x32x16_bf16 v[192:207], v[232:235], v[144:147], v[192:207]\n"
    "ds_read_b64_tr_b16 v[224:225], v48 offset:24576\n"
    "ds_read_b64_tr_b16 v[226:227], v48 offset:26624\n"
    "s_waitcnt lgkmcnt(10)\n"
    "v_mfma_f32_32x32x16_bf16 v[208:223], v[236:239], v[144:147], v[208:223]\n"
    "ds_read_b64_tr_b16 v[228:229], v49 offset:24576\n"
    "ds_read_b64_tr_b16 v[230:231], v49 offset:26624\n"
    "s_waitcnt lgkmcnt(10)\n"
    "v_mfma_f32_32x32x16_bf16 v[160:175], v[240:243], v[148:151], v[160:175]\n"
    "ds_read_b64_tr_b16 v[232:233], v50 offset:24576\n"
    "ds_read_b64_tr_b16 v[234:235], v50 offset:26624\n"
    "s_waitcnt lgkmcnt(10)\n"
    "v_mfma_f32_32x32x16_bf16 v[176:191], v[244:247], v[148:151], v[176:191]\n"
    "ds_read_b64_tr_b16 v[236:237], v51 offset:24576\n"
    "ds_read_b64_tr_b16 v[238:239], v51 offset:26624\n"
    "s_waitcnt lgkmcnt(10)\n"
    "v_mfma_f32_32x32x16_bf16 v[192:207], v[248:251], v[148:151], v[192:207]\n"
    "ds_read_b64_tr_b16 v[240:241], v48 offset:28672\n"
    "ds_read_b64_tr_b16 v[242:243], v48 offset:30720\n"
    "s_waitcnt lgkmcnt(10)\n"
    "v_mfma_f32_32x32x16_bf16 v[208:223], v[252:255], v[148:151], v[208:223]\n"
    "ds_read_b64_tr_b16 v[244:245], v49 offset:28672\n"
    "ds_read_b64_tr_b16 v[246:247], v49 offset:30720\n"
    "s_waitcnt lgkmcnt(10)\n"
    "v_mfma_f32_32x32x16_bf16 v[160:175], v[224:227], v[152:155], v[160:175]\n"
    "ds_read_b64_tr_b16 v[248:249], v50 offset:28672\n"
    "ds_read_b64_tr_b16 v[250:251], v50 offset:30720\n"
    "s_waitcnt lgkmcnt(10)\n"
    "v_mfma_f32_32x32x16_bf16 v[176:191], v[228:231], v[152:155], v[176:191]\n"
    "ds_read_b64_tr_b16 v[252:253], v51 offset:28672\n"
    "ds_read_b64_tr_b16 v[254:255], v51 offset:30720\n"
    "s_waitcnt lgkmcnt(10)\n"
    "v_mfma_f32_32x32x16_bf16 v[192:207], v[232:235], v[152:155], v[192:207]\n"
    "s_waitcnt lgkmcnt(8)\n"
    "v_mfma_f32_32x32x16_bf16 v[208:223], v[236:239], v[152:155], v[208:223]\n"
    "s_waitcnt lgkmcnt(6)\n"
    "v_mfma_f32_32x32x16_bf16 v[160:175], v[240:243], v[156:159], v[160:175]\n"
    "s_waitcnt lgkmcnt(4)\n"
    "v_mfma_f32_32x32x16_bf16 v[176:191], v[244:247], v[156:159], v[176:191]\n"
    "s_waitcnt lgkmcnt(2)\n"
    "v_mfma_f32_32x32x16_bf16 v[192:207], v[248:251], v[156:159], v[192:207]\n"
    "s_waitcnt lgkmcnt(0)\n"
    "v_mfma_f32_32x32x16_bf16 v[208:223], v[252:255], v[156:159], v[208:223]\n"
    "s_barrier\n"
    "s_sleep 8\n"
    "buffer_load_dwordx2 v[10:11], v62, s[44:47], 0 offen sc1\n"
    "ds_read_b64_tr_b16 v[224:225], v48 offset:32768\n"
    "ds_read_b64_tr_b16 v[226:227], v48 offset:34816\n"
    "ds_read_b64_tr_b16 v[228:229], v49 offset:32768\n"
    "ds_read_b64_tr_b16 v[230:231], v49 offset:34816\n"
    "ds_read_b64_tr_b16 v[232:233], v50 offset:32768\n"
    "ds_read_b64_tr_b16 v[234:235], v50 offset:34816\n"
    "ds_read_b64_tr_b16 v[236:237], v51 offset:32768\n"
    "ds_read_b64_tr_b16 v[238:239], v51 offset:34816\n"
    "ds_read_b64_tr_b16 v[240:241], v48 offset:36864\n"
    "ds_read_b64_tr_b16 v[242:243], v48 offset:38912\n"
    "ds_read_b64_tr_b16 v[244:245], v49 offset:36864\n"
    "ds_read_b64_tr_b16 v[246:247], v49 offset:38912\n"
    "s_waitcnt lgkmcnt(0)\n"
    "s_mov_b32 s93, 262144\n"
    "L_p1_5_%=:\n"
    "s_waitcnt vmcnt(0)\n"
    "v_cmp_eq_u64 vcc, v[10:11], s[62:63]\n"
    "s_nop 1\n"
    "s_and_b64 vcc, vcc, 7\n"
    "s_cmp_eq_u64 vcc, 7\n"
    "s_cbranch_scc1 L_p1d_6_%=\n"
    "s_andn2_b64 exec, 7, vcc\n"
    "buffer_load_dwordx2 v[10:11], v62, s[44:47], 0 offen sc1\n"
    "s_mov_b64 exec, -1\n"
    "s_sub_u32 s93, s93, 1\n"
    "s_cmp_lg_u32 s93, 0\n"
    "s_cbranch_scc1 L_p1_5_%=\n"
    "s_waitcnt vmcnt(0)\n"
    "L_p1d_6_%=:\n"
    "; @reset\n"
    "buffer_load_dwordx4 v[64:67], v60, s[44:47], s70 offen sc1\n"
    "buffer_load_dwordx4 v[68:71], v60, s[44:47], s70 offen offset:1024 sc1\n"
    "buffer_load_dwordx4 v[72:75], v60, s[44:47], s70 offen offset:2048 sc1\n"
    "buffer_load_dwordx4 v[76:79], v60, s[44:47], s70 offen offset:3072 sc1\n"
    "buffer_load_dwordx4 v[80:83], v60, s[44:47], s71 offen sc1\n"
    "buffer_load_dwordx4 v[84:87], v60, s[44:47], s71 offen offset:1024 sc1\n"
    "buffer_load_dwordx4 v[88:91], v60, s[44:47], s71 offen offset:2048 sc1\n"
    "buffer_load_dwordx4 v[92:95], v60, s[44:47], s71 offen offset:3072 sc1\n"
    "buffer_load_dwordx4 v[96:99], v60, s[44:47], s72 offen sc1\n"
    "buffer_load_dwordx4 v[100:103], v60, s[44:47], s72 offen offset:1024 sc1\n"
    "buffer_load_dwordx4 v[104:107], v60, s[44:47], s72 offen offset:2048 sc1\n"
    "buffer_load_dwordx4 v[108:111], v60, s[44:47], s72 offen offset:3072 sc1\n"
    "buffer_load_dwordx4 v[112:115], v60, s[44:47], s73 offen sc1\n"
    "buffer_load_dwordx4 v[116:119], v60, s[44:47], s73 offen offset:1024 sc1\n"
    "buffer_load_dwordx4 v[120:123], v60, s[44:47], s73 offen offset:2048 sc1\n"
    "buffer_load_dwordx4 v[124:127], v60, s[44:47], s73 offen offset:3072 sc1\n"
    "buffer_load_dwordx4 v[128:131], v60, s[44:47], s74 offen sc1\n"
    "buffer_load_dwordx4 v[132:135], v60, s[44:47], s74 offen offset:1024 sc1\n"
    "buffer_load_dwordx4 v[136:139], v60, s[44:47], s74 offen offset:2048 sc1\n"
    "buffer_load_dwordx4 v[140:143], v60, s[44:47], s74 offen offset:3072 sc1\n"
    "buffer_load_dwordx4 v[144:147], v60, s[44:47], s75 offen sc1\n"
    "buffer_load_dwordx4 v[148:151], v60, s[44:47], s75 offen offset:1024 sc1\n"
    "buffer_load_dwordx4 v[152:155], v60, s[44:47], s75 offen offset:2048 sc1\n"
    "buffer_load_dwordx4 v[156:159], v60, s[44:47], s75 offen offset:3072 sc1\n"
    "s_waitcnt vmcnt(23)\n"
    "v_mfma_f32_32x32x16_bf16 v[160:175], v[224:227], v[64:67], v[160:175]\n"
    "ds_read_b64_tr_b16 v[248:249], v50 offset:36864\n"
    "ds_read_b64_tr_b16 v[250:251], v50 offset:38912\n"
    "v_mfma_f32_32x32x16_bf16 v[176:191], v[228:231], v[64:67], v[176:191]\n"
    "ds_read_b64_tr_b16 v[252:253], v51 offset:36864\n"
    "ds_read_b64_tr_b16 v[254:255], v51 offset:38912\n"
    "v_mfma_f32_32x32x16_bf16 v[192:207], v[232:235], v[64:67], v[192:207]\n"
    "ds_read_b64_tr_b16 v[224:225], v48 offset:40960\n"
    "ds_read_b64_tr_b16 v[226:227], v48 offset:43008\n"
    "v_mfma_f32_32x32x16_bf16 v[208:223], v[236:239], v[64:67], v[208:223]\n"
    "ds_read_b64_tr_b16 v[228:229], v49 offset:40960\n"
    "ds_read_b64_tr_b16 v[230:231], v49 offset:43008\n"
    "s_waitcnt vmcnt(22)\n"
    "v_mfma_f32_32x32x16_bf16 v[160:175], v[240:243], v[68:71], v[160:175]\n"
    "ds_read_b64_tr_b16 v[232:233], v50 offset:40960\n"
    "ds_read_b64_tr_b16 v[234:235], v50 offset:43008\n"
    "v_mfma_f32_32x32x16_bf16 v[176:191], v[244:247], v[68:71], v[176:191]\n"
    "ds_read_b64_tr_b16 v[236:237], v51 offset:40960\n"
    "ds_read_b64_tr_b16 v[238:239], v51 offset:43008\n"
    "s_waitcnt lgkmcnt(10)\n"
    "v_mfma_f32_32x32x16_bf16 v[192:207], v[248:251], v[68:71], v[192:207]\n"
    "ds_read_b64_tr_b16 v[240:241], v48 offset:45056\n"
    "ds_read_b64_tr_b16 v[242:243], v48 offset:47104\n"
    "s_waitcnt lgkmcnt(10)\n"
    "v_mfma_f32_32x32x16_bf16 v[208:223], v[252:255], v[68:71], v[208:223]\n"
    "ds_read_b64_tr_b16 v[244:245], v49 offset:45056\n"
    "ds_read_b64_tr_b16 v[246:247], v49 offset:47104\n"
    "s_waitcnt vmcnt(21) lgkmcnt(10)\n"
    "v_mfma_f32_32x32x16_bf16 v[160:175], v[224:227], v[72:75], v[160:175]\n"
    "ds_read_b64_tr_b16 v[248:249], v50 offset:45056\n"
    "ds_read_b64_tr_b16 v[250:251], v50 offset:47104\n"
    "s_waitcnt lgkmcnt(10)\n"
    "v_mfma_f32_32x32x16_bf16 v[176:191], v[228:231], v[72:75], v[176:191]\n"
    "ds_read_b64_tr_b16 v[252:253], v51 offset:45056\n"
    "ds_read_b64_tr_b16 v[254:255], v51 offset:47104\n"
    "s_waitcnt lgkmcnt(10)\n"
    "v_mfma_f32_32x32x16_bf16 v[192:207], v[232:235], v[72:75], v[192:207]\n"
    "ds_read_b64_tr_b16 v[224:225], v48 offset:49152\n"
    "ds_read_b64_tr_b16 v[226:227], v48 offset:51200\n"
    "s_waitcnt lgkmcnt(10)\n"
    "v_mfma_f32_32x32x16_bf16 v[208:223], v[236:239], v[72:75], v[208:223]\n"
    "ds_read_b64_tr_b16 v[228:229], v49 offset:49152\n"
    "ds_read_b64_tr_b16 v[230:231], v49 offset:51200\n"
    "s_waitcnt vmcnt(20) lgkmcnt(10)\n"
    "v_mfma_f32_32x32x16_bf16 v[160:175], v[240:243], v[76:79], v[160:175]\n"
    "ds_read_b64_tr_b16 v[232:233], v50 offset:49152\n"
    "ds_read_b64_tr_b16 v[234:235], v50 offset:51200\n"
    "s_waitcnt lgkmcnt(10)\n"
    "v_mfma_f32_32x32x16_bf16 v[176:191], v[244:247], v[76:79], v[176:191]\n"
    "ds_read_b64_tr_b16 v[236:237], v51 offset:49152\n"
    "ds_read_b64_tr_b16 v[238:239], v51 offset:51200\n"
    "s_waitcnt lgkmcnt(10)\n"
    "v_mfma_f32_32x32x16_bf16 v[192:207], v[248:251], v[76:79], v[192:207]\n"
    "ds_read_b64_tr_b16 v[240:241], v48 offset:53248\n"
    "ds_read_b64_tr_b16 v[242:243], v48 offset:55296\n"
    "s_waitcnt lgkmcnt(10)\n"
    "v_mfma_f32_32x32x16_bf16 v[208:223], v[252:255], v[76:79], v[208:223]\n"
    "ds_read_b64_tr_b16 v[244:245], v49 offset:53248\n"
    "ds_read_b64_tr_b16 v[246:247], v49 offset:55296\n"
    "s_waitcnt vmcnt(19) lgkmcnt(10)\n"
    "v_mfma_f32_32x32x16_bf16 v[160:175], v[224:227], v[80:83], v[160:175]\n"
    "ds_read_b64_tr_b16 v[248:249], v50 offset:53248\n"
    "ds_read_b64_tr_b16 v[250:251], v50 offset:55296\n"
    "s_waitcnt lgkmcnt(10)\n"
    "v_mfma_f32_32x32x16_bf16 v[176:191], v[228:231], v[80:83], v[176:191]\n"
    "ds_read_b64_tr_b16 v[252:253], v51 offset:53248\n"
    "ds_read_b64_tr_b16 v[254:255], v51 offset:55296\n"
    "s_waitcnt lgkmcnt(10)\n"
    "v_mfma_f32_32x32x16_bf16 v[192:207], v[232:235], v[80:83], v[192:207]\n"
    "ds_read_b64_tr_b16 v[224:225], v48 offset:57344\n"
    "ds_read_b64_tr_b16 v[226:227], v48 offset:59392\n"
    "s_waitcnt lgkmcnt(10)\n"
    "v_mfma_f32_32x32x16_bf16 v[208:223], v[236:239], v[80:83], v[208:223]\n"
    "ds_read_b64_tr_b16 v[228:229], v49 offset:57344\n"
    "ds_read_b64_tr_b16 v[230:231], v49 offset:59392\n"
    "s_waitcnt vmcnt(18) lgkmcnt(10)\n"
    "v_mfma_f32_32x32x16_bf16 v[160:175], v[240:243], v[84:87], v[160:175]\n"
    "ds_read_b64_tr_b16 v[232:233], v50 offset:57344\n"
    "ds_read_b64_tr_b16 v[234:235], v50 offset:59392\n"
    "s_waitcnt lgkmcnt(10)\n"
    "v_mfma_f32_32x32x16_bf16 v[176:191], v[244:247], v[84:87], v[176:191]\n"
    "ds_read_b64_tr_b16 v[236:237], v51 offset:57344\n"
    "ds_read_b64_tr_b16 v[238:239], v51 offset:59392\n"
    "s_waitcnt lgkmcnt(10)\n"
    "v_mfma_f32_32x32x16_bf16 v[192:207], v[248:251], v[84:87], v[192:207]\n"
    "ds_read_b64_tr_b16 v[240:241], v48 offset:61440\n"
    "ds_read_b64_tr_b16 v[242:243], v48 offset:63488\n"
    "s_waitcnt lgkmcnt(10)\n"
    "v_mfma_f32_32x32x16_bf16 v[208:223], v[252:255], v[84:87], v[208:223]\n"
    "ds_read_b64_tr_b16 v[244:245], v49 offset:61440\n"
    "ds_read_b64_tr_b16 v[246:247], v49 offset:63488\n"
    "s_waitcnt vmcnt(17) lgkmcnt(10)\n"
    "v_mfma_f32_32x32x16_bf16 v[160:175], v[224:227], v[88:91], v[160:175]\n"
    "ds_read_b64_tr_b16 v[248:249], v50 offset:61440\n"
    "ds_read_b64_tr_b16 v[250:251], v50 offset:63488\n"
    "s_waitcnt lgkmcnt(10)\n"
    "v_mfma_f32_32x32x16_bf16 v[176:191], v[228:231], v[88:91], v[176:191]\n"
    "ds_read_b64_tr_b16 v[252:253], v51 offset:61440\n"
    "ds_read_b64_tr_b16 v[254:255], v51 offset:63488\n"
    "s_waitcnt lgkmcnt(10)\n"
    "v_mfma_f32_32x32x16_bf16 v[192:207], v[232:235], v[88:91], v[192:207]\n"
    "ds_read_b64_tr_b16 v[224:225], v52 offset:0\n"
    "ds_read_b64_tr_b16 v[226:227], v52 offset:2048\n"
    "s_waitcnt lgkmcnt(10)\n"
    "v_mfma_f32_32x32x16_bf16 v[208:223], v[236:239], v[88:91], v[208:223]\n"
    "ds_read_b64_tr_b16 v[228:229], v52 offset:4096\n"
    "ds_read_b64_tr_b16 v[230:231], v52 offset:6144\n"
    "s_waitcnt vmcnt(16) lgkmcnt(10)\n"
    "v_mfma_f32_32x32x16_bf16 v[160:175], v[240:243], v[92:95], v[160:175]\n"
    "ds_read_b64_tr_b16 v[232:233], v52 offset:8192\n"
    "ds_read_b64_tr_b16 v[234:235], v52 offset:10240\n"
    "s_waitcnt lgkmcnt(10)\n"
    "v_mfma_f32_32x32x16_bf16 v[176:191], v[244:247], v[92:95], v[176:191]\n"
    "ds_read_b64_tr_b16 v[236:237], v52 offset:12288\n"
    "ds_read_b64_tr_b16 v[238:239], v52 offset:14336\n"
    "s_waitcnt lgkmcnt(10)\n"
    "v_mfma_f32_32x32x16_bf16 v[192:207], v[248:251], v[92:95], v[192:207]\n"
    "ds_read_b64_tr_b16 v[240:241], v52 offset:16384\n"
    "ds_read_b64_tr_b16 v[242:243], v52 offset:18432\n"
    "s_waitcnt lgkmcnt(10)\n"
    "v_mfma_f32_32x32x16_bf16 v[208:223], v[252:255], v[92:95], v[208:223]\n"
    "ds_read_b64_tr_b16 v[244:245], v52 offset:20480\n"
    "ds_read_b64_tr_b16 v[246:247], v52 offset:22528\n"
    "s_waitcnt vmcnt(15) lgkmcnt(10)\n"
    "v_mfma_f32_32x32x16_bf16 v[160:175], v[224:227], v[96:99], v[160:175]\n"
    "ds_read_b64_tr_b16 v[248:249], v52 offset:24576\n"
    "ds_read_b64_tr_b16 v[250:251], v52 offset:26624\n"
    "s_waitcnt vmcnt(14) lgkmcnt(10)\n"
    "v_mfma_f32_32x32x16_bf16 v[160:175], v[228:231], v[100:103], v[160:175]\n"
    "ds_read_b64_tr_b16 v[252:253], v52 offset:28672\n"
    "ds_read_b64_tr_b16 v[254:255], v52 offset:30720\n"
    "s_waitcnt vmcnt(13) lgkmcnt(10)\n"
    "v_mfma_f32_32x32x16_bf16 v[160:175], v[232:235], v[104:107], v[160:175]\n"
    "ds_read_b64_tr_b16 v[224:225], v52 offset:32768\n"
    "ds_read_b64_tr_b16 v[226:227], v52 offset:34816\n"
    "s_waitcnt vmcnt(12) lgkmcnt(10)\n"
    "v_mfma_f32_32x32x16_bf16 v[160:175], v[236:239], v[108:111], v[160:175]\n"
    "ds_read_b64_tr_b16 v[228:229], v52 offset:36864\n"
    "ds_read_b64_tr_b16 v[230:231], v52 offset:38912\n"
    "s_waitcnt vmcnt(11) lgkmcnt(10)\n"
    "v_mfma_f32_32x32x16_bf16 v[160:175], v[240:243], v[112:115], v[160:175]\n"
    "ds_read_b64_tr_b16 v[232:233], v52 offset:40960\n"
    "ds_read_b64_tr_b16 v[234:235], v52 offset:43008\n"
    "s_waitcnt vmcnt(10) lgkmcnt(10)\n"
    "v_mfma_f32_32x32x16_bf16 v[160:175], v[244:247], v[116:119], v[160:175]\n"
    "ds_read_b64_tr_b16 v[236:237], v52 offset:45056\n"
    "ds_read_b64_tr_b16 v[238:239], v52 offset:47104\n"
    "s_waitcnt vmcnt(9) lgkmcnt(10)\n"
    "v_mfma_f32_32x32x16_bf16 v[160:175], v[248:251], v[120:123], v[160:175]\n"
    "ds_read_b64_tr_b16 v[240:241], v52 offset:49152\n"
    "ds_read_b64_tr_b16 v[242:243], v52 offset:51200\n"
    "s_waitcnt vmcnt(8) lgkmcnt(10)\n"
    "v_mfma_f32_32x32x16_bf16 v[160:175], v[252:255], v[124:127], v[160:175]\n"
    "ds_read_b64_tr_b16 v[244:245], v52 offset:53248\n"
    "ds_read_b64_tr_b16 v[246:247], v52 offset:55296\n"
    "s_waitcnt vmcnt(7) lgkmcnt(10)\n"
    "v_mfma_f32_32x32x16_bf16 v[160:175], v[224:227], v[128:131], v[160:175]\n"
    "ds_read_b64_tr_b16 v[248:249], v52 offset:57344\n"
    "ds_read_b64_tr_b16 v[250:251], v52 offset:59392\n"
    "s_waitcnt vmcnt(6) lgkmcnt(10)\n"
    "v_mfma_f32_32x32x16_bf16 v[160:175], v[228:231], v[132:135], v[160:175]\n"
    "ds_read_b64_tr_b16 v[252:253], v52 offset:61440\n"
    "ds_read_b64_tr_b16 v[254:255], v52 offset:63488\n"
    "s_waitcnt vmcnt(5) lgkmcnt(10)\n"
    "v_mfma_f32_32x32x16_bf16 v[160:175], v[232:235], v[136:139], v[160:175]\n"
    "ds_read_b64_tr_b16 v[224:225], v53 offset:0\n"
    "ds_read_b64_tr_b16 v[226:227], v53 offset:2048\n"
    "s_waitcnt vmcnt(4) lgkmcnt(10)\n"
    "v_mfma_f32_32x32x16_bf16 v[160:175], v[236:239], v[140:143], v[160:175]\n"
    "ds_read_b64_tr_b16 v[228:229], v53 offset:4096\n"
    "ds_read_b64_tr_b16 v[230:231], v53 offset:6144\n"
    "s_waitcnt vmcnt(3) lgkmcnt(10)\n"
    "v_mfma_f32_32x32x16_bf16 v[160:175], v[240:243], v[144:147], v[160:175]\n"
    "ds_read_b64_tr_b16 v[232:233], v53 offset:8192\n"
    "ds_read_b64_tr_b16 v[234:235], v53 offset:10240\n"
    "s_waitcnt vmcnt(2) lgkmcnt(10)\n"
    "v_mfma_f32_32x32x16_bf16 v[160:175], v[244:247], v[148:151], v[160:175]\n"
    "ds_read_b64_tr_b16 v[236:237], v53 offset:12288\n"
    "ds_read_b64_tr_b16 v[238:239], v53 offset:14336\n"
    "s_waitcnt vmcnt(1) lgkmcnt(10)\n"
    "v_mfma_f32_32x32x16_bf16 v[160:175], v[248:251], v[152:155], v[160:175]\n"
    "ds_read_b64_tr_b16 v[240:241], v53 offset:16384\n"
    "ds_read_b64_tr_b16 v[242:243], v53 offset:18432\n"
    "s_waitcnt vmcnt(0) lgkmcnt(10)\n"
    "v_mfma_f32_32x32x16_bf16 v[160:175], v[252:255], v[156:159], v[160:175]\n"
    "ds_read_b64_tr_b16 v[244:245], v53 offset:20480\n"
    "ds_read_b64_tr_b16 v[246:247], v53 offset:22528\n"
    "s_waitcnt lgkmcnt(10)\n"
    "v_mfma_f32_32x32x16_bf16 v[176:191], v[224:227], v[96:99], v[176:191]\n"
    "ds_read_b64_tr_b16 v[248:249], v53 offset:24576\n"
    "ds_read_b64_tr_b16 v[250:251], v53 offset:26624\n"
    "s_waitcnt lgkmcnt(10)\n"
    "v_mfma_f32_32x32x16_bf16 v[176:191], v[228:231], v[100:103], v[176:191]\n"
    "ds_read_b64_tr_b16 v[252:253], v53 offset:28672\n"
    "ds_read_b64_tr_b16 v[254:255], v53 offset:30720\n"
    "s_waitcnt lgkmcnt(10)\n"
    "v_mfma_f32_32x32x16_bf16 v[176:191], v[232:235], v[104:107], v[176:191]\n"
    "ds_read_b64_tr_b16 v[224:225], v53 offset:32768\n"
    "ds_read_b64_tr_b16 v[226:227], v53 offset:34816\n"
    "s_waitcnt lgkmcnt(10)\n"
    "v_mfma_f32_32x32x16_bf16 v[176:191], v[236:239], v[108:111], v[176:191]\n"
    "ds_read_b64_tr_b16 v[228:229], v53 offset:36864\n"
    "ds_read_b64_tr_b16 v[230:231], v53 offset:38912\n"
    "v_cvt_pk_bf16_f32 v4, v160, v161\n"
    "v_cvt_pk_bf16_f32 v5, v162, v163\n"
    "v_cvt_pk_bf16_f32 v6, v164, v165\n"
    "v_cvt_pk_bf16_f32 v7, v166, v167\n"
    "buffer_store_dwordx4 v[4:7], v60, s[44:47], s78 offen sc1\n"
    "s_waitcnt lgkmcnt(10)\n"
    "v_mfma_f32_32x32x16_bf16 v[176:191], v[240:243], v[112:115], v[176:191]\n"
    "ds_read_b64_tr_b16 v[232:233], v53 offset:40960\n"
    "ds_read_b64_tr_b16 v[234:235], v53 offset:43008\n"
    "v_cvt_pk_bf16_f32 v8, v168, v169\n"
    "v_cvt_pk_bf16_f32 v9, v170, v171\n"
    "v_cvt_pk_bf16_f32 v10, v172, v173\n"
    "v_cvt_pk_bf16_f32 v11, v174, v175\n"
    "buffer_store_dwordx4 v[8:11], v60, s[44:47], s78 offen offset:1024 sc1\n"
    "s_waitcnt lgkmcnt(10)\n"
    "v_mfma_f32_32x32x16_bf16 v[176:191], v[244:247], v[116:119], v[176:191]\n"
    "ds_read_b64_tr_b16 v[236:237], v53 offset:45056\n"
    "ds_read_b64_tr_b16 v[238:239], v53 offset:47104\n"
    "s_waitcnt lgkmcnt(10)\n"
    "v_mfma_f32_32x32x16_bf16 v[176:191], v[248:251], v[120:123], v[176:191]\n"
    "ds_read_b64_tr_b16 v[240:241], v53 offset:49152\n"
    "ds_read_b64_tr_b16 v[242:243], v53 offset:51200\n"
    "s_waitcnt lgkmcnt(10)\n"
    "v_mfma_f32_32x32x16_bf16 v[176:191], v[252:255], v[124:127], v[176:191]\n"
    "ds_read_b64_tr_b16 v[244:245], v53 offset:53248\n"
    "ds_read_b64_tr_b16 v[246:247], v53 offset:55296\n"
    "s_waitcnt lgkmcnt(10)\n"
    "v_mfma_f32_32x32x16_bf16 v[176:191], v[224:227], v[128:131], v[176:191]\n"
    "ds_read_b64_tr_b16 v[248:249], v53 offset:57344\n"
    "ds_read_b64_tr_b16 v[250:251], v53 offset:59392\n"
    "s_waitcnt lgkmcnt(10)\n"
    "v_mfma_f32_32x32x16_bf16 v[176:191], v[228:231], v[132:135], v[176:191]\n"
    "ds_read_b64_tr_b16 v[252:253], v53 offset:61440\n"
    "ds_read_b64_tr_b16 v[254:255], v53 offset:63488\n"
    "s_waitcnt lgkmcnt(10)\n"
    "v_mfma_f32_32x32x16_bf16 v[176:191], v[232:235], v[136:139], v[176:191]\n"
    "ds_read_b64_tr_b16 v[224:225], v54 offset:0\n"
    "ds_read_b64_tr_b16 v[226:227], v54 offset:2048\n"
    "s_waitcnt lgkmcnt(10)\n"
    "v_mfma_f32_32x32x16_bf16 v[176:191], v[236:239], v[140:143], v[176:191]\n"
    "ds_read_b64_tr_b16 v[228:229], v54 offset:4096\n"
    "ds_read_b64_tr_b16 v[230:231], v54 offset:6144\n"
    "s_waitcnt lgkmcnt(10)\n"
    "v_mfma_f32_32x32x16_bf16 v[176:191], v[240:243], v[144:147], v[176:191]\n"
    "ds_read_b64_tr_b16 v[232:233], v54 offset:8192\n"
    "ds_read_b64_tr_b16 v[234:235], v54 offset:10240\n"
    "s_waitcnt lgkmcnt(10)\n"
    "v_mfma_f32_32x32x16_bf16 v[176:191], v[244:247], v[148:151], v[176:191]\n"
    "ds_read_b64_tr_b16 v[236:237], v54 offset:12288\n"
    "ds_read_b64_tr_b16 v[238:239], v54 offset:14336\n"
    "s_waitcnt lgkmcnt(10)\n"
    "v_mfma_f32_32x32x16_bf16 v[176:191], v[248:251], v[152:155], v[176:191]\n"
    "ds_read_b64_tr_b16 v[240:241], v54 offset:16384\n"
    "ds_read_b64_tr_b16 v[242:243], v54 offset:18432\n"
    "s_waitcnt lgkmcnt(10)\n"
    "v_mfma_f32_32x32x16_bf16 v[176:191], v[252:255], v[156:159], v[176:191]\n"
    "ds_read_b64_tr_b16 v[244:245], v54 offset:20480\n"
    "ds_read_b64_tr_b16 v[246:247], v54 offset:22528\n"
    "s_waitcnt lgkmcnt(10)\n"
    "v_mfma_f32_32x32x16_bf16 v[192:207], v[224:227], v[96:99], v[192:207]\n"
    "ds_read_b64_tr_b16 v[248:249], v54 offset:24576\n"
    "ds_read_b64_tr_b16 v[250:251], v54 offset:26624\n"
    "s_waitcnt lgkmcnt(10)\n"
    "v_mfma_f32_32x32x16_bf16 v[192:207], v[228:231], v[100:103], v[192:207]\n"
    "ds_read_b64_tr_b16 v[252:253], v54 offset:28672\n"
    "ds_read_b64_tr_b16 v[254:255], v54 offset:30720\n"
    "s_waitcnt lgkmcnt(10)\n"
    "v_mfma_f32_32x32x16_bf16 v[192:207], v[232:235], v[104:107], v[192:207]\n"
    "ds_read_b64_tr_b16 v[224:225], v54 offset:32768\n"
    "ds_read_b64_tr_b16 v[226:227], v54 offset:34816\n"
    "s_waitcnt lgkmcnt(10)\n"
    "v_mfma_f32_32x32x16_bf16 v[192:207], v[236:239], v[108:111], v[192:207]\n"
    "ds_read_b64_tr_b16 v[228:229], v54 offset:36864\n"
    "ds_read_b64_tr_b16 v[230:231], v54 offset:38912\n"
    "v_cvt_pk_bf16_f32 v16, v176, v177\n"
    "v_cvt_pk_bf16_f32 v17, v178, v179\n"
    "v_cvt_pk_bf16_f32 v18, v180, v181\n"
    "v_cvt_pk_bf16_f32 v19, v182, v183\n"
    "buffer_store_dwordx4 v[16:19], v60, s[44:47], s78 offen offset:2048 sc1\n"
    "s_waitcnt lgkmcnt(10)\n"
    "v_mfma_f32_32x32x16_bf16 v[192:207], v[240:243], v[112:115], v[192:207]\n"
    "ds_read_b64_tr_b16 v[232:233], v54 offset:40960\n"
    "ds_read_b64_tr_b16 v[234:235], v54 offset:43008\n"
    "v_cvt_pk_bf16_f32 v20, v184, v185\n"
    "v_cvt_pk_bf16_f32 v21, v186, v187\n"
    "v_cvt_pk_bf16_f32 v22, v188, v189\n"
    "v_cvt_pk_bf16_f32 v23, v190, v191\n"
    "buffer_store_dwordx4 v[20:23], v60, s[44:47], s78 offen offset:3072 sc1\n"
    "s_waitcnt lgkmcnt(10)\n"
    "v_mfma_f32_32x32x16_bf16 v[192:207], v[244:247], v[116:119], v[192:207]\n"
    "ds_read_b64_tr_b16 v[236:237], v54 offset:45056\n"
    "ds_read_b64_tr_b16 v[238:239], v54 offset:47104\n"
    "s_waitcnt lgkmcnt(10)\n"
    "v_mfma_f32_32x32x16_bf16 v[192:207], v[248:251], v[120:123], v[192:207]\n"
    "ds_read_b64_tr_b16 v[240:241], v54 offset:49152\n"
    "ds_read_b64_tr_b16 v[242:243], v54 offset:51200\n"
    "s_waitcnt lgkmcnt(10)\n"
    "v_mfma_f32_32x32x16_bf16 v[192:207], v[252:255], v[124:127], v[192:207]\n"
    "ds_read_b64_tr_b16 v[244:245], v54 offset:53248\n"
    "ds_read_b64_tr_b16 v[246:247], v54 offset:55296\n"
    "s_waitcnt lgkmcnt(10)\n"
    "v_mfma_f32_32x32x16_bf16 v[192:207], v[224:227], v[128:131], v[192:207]\n"
    "ds_read_b64_tr_b16 v[248:249], v54 offset:57344\n"
    "ds_read_b64_tr_b16 v[250:251], v54 offset:59392\n"
    "s_waitcnt lgkmcnt(10)\n"
    "v_mfma_f32_32x32x16_bf16 v[192:207], v[228:231], v[132:135], v[192:207]\n"
    "ds_read_b64_tr_b16 v[252:253], v54 offset:61440\n"
    "ds_read_b64_tr_b16 v[254:255], v54 offset:63488\n"
    "s_waitcnt lgkmcnt(10)\n"
    "v_mfma_f32_32x32x16_bf16 v[192:207], v[232:235], v[136:139], v[192:207]\n"
    "ds_read_b64_tr_b16 v[224:225], v55 offset:0\n"
    "ds_read_b64_tr_b16 v[226:227], v55 offset:2048\n"
    "s_waitcnt lgkmcnt(10)\n"
    "v_mfma_f32_32x32x16_bf16 v[192:207], v[236:239], v[140:143], v[192:207]\n"
    "ds_read_b64_tr_b16 v[228:229], v55 offset:4096\n"
    "ds_read_b64_tr_b16 v[230:231], v55 offset:6144\n"
    "s_waitcnt lgkmcnt(10)\n"
    "v_mfma_f32_32x32x16_bf16 v[192:207], v[240:243], v[144:147], v[192:207]\n"
    "ds_read_b64_tr_b16 v[232:233], v55 offset:8192\n"
    "ds_read_b64_tr_b16 v[234:235], v55 offset:10240\n"
    "s_waitcnt lgkmcnt(10)\n"
    "v_mfma_f32_32x32x16_bf16 v[192:207], v[244:247], v[148:151], v[192:207]\n"
    "ds_read_b64_tr_b16 v[236:237], v55 offset:12288\n"
    "ds_read_b64_tr_b16 v[238:239], v55 offset:14336\n"
    "s_waitcnt lgkmcnt(10)\n"
    "v_mfma_f32_32x32x16_bf16 v[192:207], v[248:251], v[152:155], v[192:207]\n"
    "ds_read_b64_tr_b16 v[240:241], v55 offset:16384\n"
    "ds_read_b64_tr_b16 v[242:243], v55 offset:18432\n"
    "s_waitcnt lgkmcnt(10)\n"
    "v_mfma_f32_32x32x16_bf16 v[192:207], v[252:255], v[156:159], v[192:207]\n"
    "ds_read_b64_tr_b16 v[244:245], v55 offset:20480\n"
    "ds_read_b64_tr_b16 v[246:247], v55 offset:22528\n"
    "s_waitcnt lgkmcnt(10)\n"
    "v_mfma_f32_32x32x16_bf16 v[208:223], v[224:227], v[96:99], v[208:223]\n"
    "ds_read_b64_tr_b16 v[248:249], v55 offset:24576\n"
    "ds_read_b64_tr_b16 v[250:251], v55 offset:26624\n"
    "s_waitcnt lgkmcnt(10)\n"
    "v_mfma_f32_32x32x16_bf16 v[208:223], v[228:231], v[100:103], v[208:223]\n"
    "ds_read_b64_tr_b16 v[252:253], v55 offset:28672\n"
    "ds_read_b64_tr_b16 v[254:255], v55 offset:30720\n"
    "s_waitcnt lgkmcnt(10)\n"
    "v_mfma_f32_32x32x16_bf16 v[208:223], v[232:235], v[104:107], v[208:223]\n"
    "ds_read_b64_tr_b16 v[224:225], v55 offset:32768\n"
    "ds_read_b64_tr_b16 v[226:227], v55 offset:34816\n"
    "s_waitcnt lgkmcnt(10)\n"
    "v_mfma_f32_32x32x16_bf16 v[208:223], v[236:239], v[108:111], v[208:223]\n"
    "ds_read_b64_tr_b16 v[228:229], v55 offset:36864\n"
    "ds_read_b64_tr_b16 v[230:231], v55 offset:38912\n"
    "v_cvt_pk_bf16_f32 v4, v192, v193\n"
    "v_cvt_pk_bf16_f32 v5, v194, v195\n"
    "v_cvt_pk_bf16_f32 v6, v196, v197\n"
    "v_cvt_pk_bf16_f32 v7, v198, v199\n"
    "buffer_store_dwordx4 v[4:7], v60, s[44:47], s79 offen sc1\n"
    "s_waitcnt lgkmcnt(10)\n"
    "v_mfma_f32_32x32x16_bf16 v[208:223], v[240:243], v[112:115], v[208:223]\n"
    "ds_read_b64_tr_b16 v[232:233], v55 offset:40960\n"
    "ds_read_b64_tr_b16 v[234:235], v55 offset:43008\n"
    "v_cvt_pk_bf16_f32 v8, v200, v201\n"
    "v_cvt_pk_bf16_f32 v9, v202, v203\n"
    "v_cvt_pk_bf16_f32 v10, v204, v205\n"
    "v_cvt_pk_bf16_f32 v11, v206, v207\n"
    "buffer_store_dwordx4 v[8:11], v60, s[44:47], s79 offen offset:1024 sc1\n"
    "s_waitcnt lgkmcnt(10)\n"
    "v_mfma_f32_32x32x16_bf16 v[208:223], v[244:247], v[116:119], v[208:223]\n"
    "ds_read_b64_tr_b16 v[236:237], v55 offset:45056\n"
    "ds_read_b64_tr_b16 v[238:239], v55 offset:47104\n"
    "s_waitcnt lgkmcnt(10)\n"
    "v_mfma_f32_32x32x16_bf16 v[208:223], v[248:251], v[120:123], v[208:223]\n"
    "ds_read_b64_tr_b16 v[240:241], v55 offset:49152\n"
    "ds_read_b64_tr_b16 v[242:243], v55 offset:51200\n"
    "s_waitcnt lgkmcnt(10)\n"
    "v_mfma_f32_32x32x16_bf16 v[208:223], v[252:255], v[124:127], v[208:223]\n"
    "ds_read_b64_tr_b16 v[244:245], v55 offset:53248\n"
    "ds_read_b64_tr_b16 v[246:247], v55 offset:55296\n"
    "s_waitcnt lgkmcnt(10)\n"
    "v_mfma_f32_32x32x16_bf16 v[208:223], v[224:227], v[128:131], v[208:223]\n"
    "ds_read_b64_tr_b16 v[248:249], v55 offset:57344\n"
    "ds_read_b64_tr_b16 v[250:251], v55 offset:59392\n"
    "s_waitcnt lgkmcnt(10)\n"
    "v_mfma_f32_32x32x16_bf16 v[208:223], v[228:231], v[132:135], v[208:223]\n"
    "ds_read_b64_tr_b16 v[252:253], v55 offset:61440\n"
    "ds_read_b64_tr_b16 v[254:255], v55 offset:63488\n"
    "s_waitcnt lgkmcnt(10)\n"
    "v_mfma_f32_32x32x16_bf16 v[208:223], v[232:235], v[136:139], v[208:223]\n"
    "s_waitcnt lgkmcnt(8)\n"
    "v_mfma_f32_32x32x16_bf16 v[208:223], v[236:239], v[140:143], v[208:223]\n"
    "s_waitcnt lgkmcnt(6)\n"
    "v_mfma_f32_32x32x16_bf16 v[208:223], v[240:243], v[144:147], v[208:223]\n"
    "s_waitcnt lgkmcnt(4)\n"
    "v_mfma_f32_32x32x16_bf16 v[208:223], v[244:247], v[148:151], v[208:223]\n"
    "s_waitcnt lgkmcnt(2)\n"
    "v_mfma_f32_32x32x16_bf16 v[208:223], v[248:251], v[152:155], v[208:223]\n"
    "s_waitcnt lgkmcnt(0)\n"
    "v_mfma_f32_32x32x16_bf16 v[208:223], v[252:255], v[156:159], v[208:223]\n"
    "s_nop 7\n"
    "s_nop 3\n"
    "v_cvt_pk_bf16_f32 v16, v208, v209\n"
    "v_cvt_pk_bf16_f32 v17, v210, v211\n"
    "v_cvt_pk_bf16_f32 v18, v212, v213\n"
    "v_cvt_pk_bf16_f32 v19, v214, v215\n"
    "buffer_store_dwordx4 v[16:19], v60, s[44:47], s79 offen offset:2048 sc1\n"
    "v_cvt_pk_bf16_f32 v20, v216, v217\n"
    "v_cvt_pk_bf16_f32 v21, v218, v219\n"
    "v_cvt_pk_bf16_f32 v22, v220, v221\n"
    "v_cvt_pk_bf16_f32 v23, v222, v223\n"
    "buffer_store_dwordx4 v[20:23], v60, s[44:47], s79 offen offset:3072 sc1\n"
    "s_waitcnt vmcnt(6)\n"
    "buffer_store_dwordx2 v[12:13], v63, s[44:47], 0 offen sc1\n"
    "s_waitcnt vmcnt(5)\n"
    "buffer_store_dwordx2 v[12:13], v63, s[44:47], 0 offen offset:512 sc1\n"
    "s_waitcnt vmcnt(4)\n"
    "buffer_store_dwordx2 v[12:13], v63, s[44:47], 0 offen offset:1024 sc1\n"
    "s_waitcnt vmcnt(3)\n"
    "buffer_store_dwordx2 v[12:13], v63, s[44:47], 0 offen offset:1536 sc1\n"
    "s_barrier\n"
    "s_endpgm\n"
    "L_codeend_%=:\n"
    :: "s"(wid), "s"(blockIdx.x), "s"(ka), "s"(ldsb)
    : "memory", "vcc", "scc", "v0", "v1", "v2", "v3", "v4", "v5", "v6", "v7", "v8", "v9", "v10", "v11", "v12", "v13", "v14", "v15", "v16", "v17", "v18", "v19", "v20", "v21", "v22", "v23", "v24", "v25", "v26", "v27", "v28", "v29", "v30", "v31", "v32", "v33", "v34", "v35", "v36", "v37", "v38", "v39", "v40", "v41", "v42", "v43", "v44", "v45", "v46", "v47", "v48", "v49", "v50", "v51", "v52", "v53", "v54", "v55", "v56", "v57", "v58", "v59", "v60", "v61", "v62", "v63", "v64", "v65", "v66", "v67", "v68", "v69", "v70", "v71", "v72", "v73", "v74", "v75", "v76", "v77", "v78", "v79", "v80", "v81", "v82", "v83", "v84", "v85", "v86", "v87", "v88", "v89", "v90", "v91", "v92", "v93", "v94", "v95", "v96", "v97", "v98", "v99", "v100", "v101", "v102", "v103", "v104", "v105", "v106", "v107", "v108", "v109", "v110", "v111", "v112", "v113", "v114", "v115", "v116", "v117", "v118", "v119", "v120", "v121", "v122", "v123", "v124", "v125", "v126", "v127", "v128", "v129", "v130", "v131", "v132", "v133", "v134", "v135", "v136", "v137", "v138", "v139", "v140", "v141", "v142", "v143", "v144", "v145", "v146", "v147", "v148", "v149", "v150", "v151", "v152", "v153", "v154", "v155", "v156", "v157", "v158", "v159", "v160", "v161", "v162", "v163", "v164", "v165", "v166", "v167", "v168", "v169", "v170", "v171", "v172", "v173", "v174", "v175", "v176", "v177", "v178", "v179", "v180", "v181", "v182", "v183", "v184", "v185", "v186", "v187", "v188", "v189", "v190", "v191", "v192", "v193", "v194", "v195", "v196", "v197", "v198", "v199", "v200", "v201", "v202", "v203", "v204", "v205", "v206", "v207", "v208", "v209", "v210", "v211", "v212", "v213", "v214", "v215", "v216", "v217", "v218", "v219", "v220", "v221", "v222", "v223", "v224", "v225", "v226", "v227", "v228", "v229", "v230", "v231", "v232", "v233", "v234", "v235", "v236", "v237", "v238", "v239", "v240", "v241", "v242", "v243", "v244", "v245", "v246", "v247", "v248", "v249", "v250", "v251", "v252", "v253", "v254", "v255", "s8", "s9", "s10", "s11", "s12", "s13", "s14", "s15", "s16", "s17", "s18", "s19", "s20", "s21", "s22", "s23", "s24", "s25", "s26", "s27", "s28", "s29", "s30", "s31", "s33", "s34", "s35", "s36", "s37", "s38", "s39", "s40", "s41", "s42", "s43", "s44", "s45", "s46", "s47", "s48", "s49", "s50", "s51", "s52", "s53", "s54", "s55", "s56", "s57", "s58", "s59", "s60", "s61", "s62", "s63", "s64", "s65", "s66", "s67", "s68", "s69", "s70", "s71", "s72", "s73", "s74", "s75", "s76", "s77", "s78", "s79", "s80", "s81", "s82", "s83", "s84", "s85", "s86", "s87", "s88", "s89", "s90", "s91", "s92", "s93", "s94", "s95", "s96", "s97", "s98", "s99");
}

extern "C" void kernel_launch(void* const* d_in, const int* in_sizes, int n_in, void* d_out, int out_size, void* d_ws, size_t ws_size, hipStream_t stream) {
  mha_block_kernel<<<256, 512, 0, stream>>>(d_in[0], d_in[1], d_in[2], d_in[3], d_in[4], d_out, d_ws);
}
```
